```python
import torch

def _softmax(x, axis=-1):
    return torch.softmax(x, dim=axis)
def _gelu(x):
    return torch.nn.functional.gelu(x, approximate="tanh")
def _silu(x):
    return torch.nn.functional.silu(x)
def _sigmoid(x):
    return torch.sigmoid(x)
def _layernorm(x, axis=-1, eps=1e-5):
    m = x.mean(dim=axis, keepdim=True)
    v = x.var(dim=axis, keepdim=True, unbiased=False)
    return (x - m) / torch.sqrt(v + eps)
def _scatter_add(tgt, idx, src, axis=0):
    return tgt.clone().index_add_(axis, idx, src)
def _segment_sum(src, seg, n):
    out = torch.zeros((n,) + src.shape[1:], dtype=src.dtype)
    return out.index_add_(0, seg, src)
def _pad(x, pw):
    flat = tuple(v for pair in reversed(pw) for v in pair)
    return torch.nn.functional.pad(x, flat)

kernel_name = 'sequential_preLN__compressed_kv_cla__dense_activation_relu__prompt'

def setup_inputs():

    _w = (lambda a: (lambda t: t.to(torch.bfloat16) if t.is_floating_point() else t)(torch.as_tensor(a)))
    def _A_setup():
        B, H, Sq, Sk, D, M = 1, 32, 2048, 2048, 128, 576
        Dc = 4 * D
        return {
            "x": _w((lambda *s: torch.randn(*s, dtype=torch.bfloat16) * 0.05)(B, Sq, M)),
            "c_kv": _w((lambda *s: torch.randn(*s, dtype=torch.bfloat16) * 0.4)(B, Sk, Dc)),
            "w_q": _w((lambda *s: torch.randn(*s, dtype=torch.bfloat16) * 3.2)(M, H, D) * (M ** -0.5)),
            "w_uk": _w((lambda *s: torch.randn(*s, dtype=torch.bfloat16) * 3.2)(Dc, H, D) * (Dc ** -0.5)),
            "w_uv": _w((lambda *s: torch.randn(*s, dtype=torch.bfloat16) * 0.4)(Dc, H, D) * (Dc ** -0.5)),
            "w_o": _w((lambda *s: torch.randn(*s, dtype=torch.bfloat16) * 0.4)(H, D, M) * ((H * D) ** -0.5)),
        }
    _A_in = _A_setup()

    def _B_setup():
        B, S, D, Dff = 1, 2048, 576, 4608
        return {
            "x": _w((lambda *s: torch.randn(*s, dtype=torch.bfloat16) * 0.05)(B, S, D)),
            "w1": _w((lambda *s: torch.randn(*s, dtype=torch.bfloat16) * 0.05)(D, Dff)),
            "w2": _w((lambda *s: torch.randn(*s, dtype=torch.bfloat16) * 0.05)(Dff, D)),
        }
    _B_in = _B_setup()

    return {
        "A_x": _A_in["x"],
        "A_c_kv": _A_in["c_kv"],
        "A_w_q": _A_in["w_q"],
        "A_w_uk": _A_in["w_uk"],
        "A_w_uv": _A_in["w_uv"],
        "A_w_o": _A_in["w_o"],
        "B_x": _B_in["x"],
        "B_w1": _B_in["w1"],
        "B_w2": _B_in["w2"],
    }

def reference(A_x, A_c_kv, A_w_q, A_w_uk, A_w_uv, A_w_o, B_x, B_w1, B_w2):
    def _A_ref(x, c_kv, w_q, w_uk, w_uv, w_o):
        B, Sq, M = x.shape[0], x.shape[1], x.shape[2]
        Sk = c_kv.shape[1]
        H, D = w_q.shape[1], w_q.shape[2]
        q = torch.einsum("bqm,mhd->bhqd", x, w_q)
        k = torch.einsum("bkc,chd->bhkd", c_kv, w_uk)
        v = torch.einsum("bkc,chd->bhkd", c_kv, w_uv)
        scores = torch.einsum("bhqd,bhkd->bhqk", q, k) * (D ** -0.5)
        off = Sk - Sq
        qi = torch.arange(Sq)[:, None]
        kj = torch.arange(Sk)[None, :]
        cmask = kj <= (qi + off)
        scores = torch.where(cmask[None, None], scores, -1e9)
        p = _softmax(scores, axis=-1)
        ctx = torch.einsum("bhqk,bhkd->bhqd", p, v)
        return torch.einsum("bhqd,hdm->bqm", ctx, w_o)

    def _B_ref(x, w1, w2):
        h = torch.einsum("bsd,df->bsf", x, w1)
        zero = torch.zeros_like(h)
        h = torch.maximum(h, zero)
        return torch.einsum("bsf,fd->bsd", h, w2)

    x = A_x
    h = x + _A_ref(_layernorm(x, axis=-1), A_c_kv, A_w_q, A_w_uk, A_w_uv, A_w_o)
    h2 = h + _B_ref(_layernorm(h, axis=-1), B_w1, B_w2)
    return h2
```

```cpp
#include <hip/hip_runtime.h>
#include <stdint.h>

#define LAS __attribute__((address_space(3)))
#define DI __device__ __forceinline__
typedef unsigned short u16;
typedef __bf16 bf16x8 __attribute__((ext_vector_type(8)));
typedef __bf16 bf16x2 __attribute__((ext_vector_type(2)));
typedef short s16x4 __attribute__((ext_vector_type(4)));
typedef short s16x8 __attribute__((ext_vector_type(8)));
typedef float f32x2 __attribute__((ext_vector_type(2)));
typedef float f32x4 __attribute__((ext_vector_type(4)));
typedef float f32x16 __attribute__((ext_vector_type(16)));
typedef unsigned u32x4 __attribute__((ext_vector_type(4)));
typedef unsigned u32x2 __attribute__((ext_vector_type(2)));

constexpr int SEQ = 2048, DM = 576, DC = 512, NH = 32, HD = 128, DFF = 4608, NQ = NH * HD;
constexpr size_t MiB = 1u << 20;
constexpr size_t WS_XN = 0, WS_K = 4 * MiB, WS_V = 20 * MiB, WS_CTX = 36 * MiB, WS_P3 = 52 * MiB, WS_H = 62 * MiB, WS_HN = 65 * MiB,
                 WS_H1 = 68 * MiB, WS_P5 = 88 * MiB;

DI unsigned cvtpk(float lo, float hi) { f32x2 v = {lo, hi}; bf16x2 b = __builtin_convertvector(v, bf16x2); return __builtin_bit_cast(unsigned, b); }
DI float bflo(unsigned w) { return __uint_as_float(w << 16); }
DI float bfhi(unsigned w) { return __uint_as_float(w & 0xffff0000u); }
typedef __amdgpu_buffer_rsrc_t srd_t;
DI srd_t make_srd(const void* p, unsigned bytes) { return __builtin_amdgcn_make_buffer_rsrc((void*)p, (short)0, (int)bytes, 0x00020000); }
DI void dma16(srd_t srd, unsigned voff, unsigned soff, unsigned m0v) {
  asm volatile("s_mov_b32 m0, %0\n\ts_nop 0\n\tbuffer_load_dwordx4 %1, %2, %3 offen lds" ::"s"(m0v), "v"(voff), "s"(srd), "s"(soff) : "memory");
}
DI bf16x8 lds_b128(LAS const char* p) { return *(LAS const bf16x8*)p; }
DI bf16x8 lds_tr2(LAS const char* p0, LAS const char* p1) {
  s16x4 a = __builtin_amdgcn_ds_read_tr16_b64_v4i16((LAS s16x4*)p0), b = __builtin_amdgcn_ds_read_tr16_b64_v4i16((LAS s16x4*)p1);
  return __builtin_bit_cast(bf16x8, (s16x8)__builtin_shufflevector(a, b, 0, 1, 2, 3, 4, 5, 6, 7));
}
#define MFMA32(a, b, c) __builtin_amdgcn_mfma_f32_32x32x16_bf16(a, b, c, 0, 0, 0)
#define MFMA16(a, b, c) __builtin_amdgcn_mfma_f32_16x16x32_bf16(a, b, c, 0, 0, 0)
#define WAIT_VM(n) asm volatile("s_waitcnt vmcnt(" #n ")" ::: "memory")
#define WAIT_LGKM0() asm volatile("s_waitcnt lgkmcnt(0)" ::: "memory")
#define BAR() __builtin_amdgcn_s_barrier()
DI float wave_sum(float v) {
#pragma unroll
  for (int o = 32; o >= 1; o >>= 1) v += __shfl_xor(v, o);
  return v;
}
DI u32x4 pack8(const f32x16& a, int b) {
  return (u32x4){cvtpk(a[b], a[b + 1]), cvtpk(a[b + 2], a[b + 3]), cvtpk(a[b + 4], a[b + 5]), cvtpk(a[b + 6], a[b + 7])};
}

constexpr int K1_SLOT = 65536;
#define SBAR() __builtin_amdgcn_sched_barrier(0)
template <bool KPART>
DI void k1_body(LAS char* lds, const u16* __restrict__ ckv, const u16* __restrict__ wk, const u16* __restrict__ wv, char* __restrict__ outp,
                int head, int rb, int wid, int lane) {
  const int wm = wid & 3;
  const unsigned ldsb = (unsigned)(size_t)lds;
  unsigned aoff[2];
#pragma unroll
  for (int par = 0; par < 2; ++par) aoff[par] = (unsigned)(lane >> 3) * 1024u + (unsigned)(((lane & 7) ^ ((4 * par + (lane >> 4)) & 7)) * 16);
  const unsigned boff = (unsigned)(lane >> 4) * 8192u + (unsigned)(((lane & 15) ^ ((lane >> 4) << 2)) * 16);
  const srd_t sa = make_srd(ckv, SEQ * DC * 2), sb = make_srd(KPART ? wk : wv, DC * NQ * 2);
  const unsigned asof = (unsigned)(rb * 256 + wid * 32) * 1024u, bsof = (unsigned)(wm * 16) * 8192u + head * 256u;
  auto stageA = [&](int kt, int slot, int p) { dma16(sa, aoff[p & 1], asof + p * 8192u + kt * 128u, ldsb + slot * K1_SLOT + wid * 4096 + p * 1024); };
  auto stageB = [&](int kt, int slot, int p) { dma16(sb, boff, bsof + (unsigned)(kt * 64 + p * 4) * 8192u, ldsb + slot * K1_SLOT + 32768 + wid * 4096 + p * 1024); };
  const int r = lane & 31, h = lane >> 5;
  const unsigned aaddr = (unsigned)(wm * 64 + r) * 128u + (unsigned)((h ^ ((r >> 1) & 7)) << 4);
  const int q = (lane & 15) >> 2, p4 = lane & 3, cb = (lane >> 4) & 1;
  const unsigned baddr = 32768u + (KPART ? 0u : 16384u) + (unsigned)(8 * h + q) * 256u + (unsigned)(q << 6) + cb * 32 + p4 * 8;
  f32x16 acc[4][2];
#pragma unroll
  for (int f = 0; f < 4; ++f)
#pragma unroll
    for (int a = 0; a < 2; ++a) acc[f][a] = (f32x16){};
  bf16x8 xa[2][2], wf[2][4];
  auto rd = [&](int slot, int s, int buf) {
    LAS const char* base = lds + slot * K1_SLOT;
#pragma unroll
    for (int a = 0; a < 2; ++a) xa[buf][a] = lds_b128(base + ((aaddr ^ (s << 5)) + a * 4096));
#pragma unroll
    for (int f = 0; f < 4; ++f) { LAS const char* p = base + ((baddr ^ (f << 6)) + s * 4096); wf[buf][f] = lds_tr2(p, p + 1024); }
  };
  auto mm = [&](int buf) {
#pragma unroll
    for (int f = 0; f < 4; ++f)
#pragma unroll
      for (int a = 0; a < 2; ++a) acc[f][a] = KPART ? MFMA32(wf[buf][f], xa[buf][a], acc[f][a]) : MFMA32(xa[buf][a], wf[buf][f], acc[f][a]);
  };
  auto tile = [&](int kt, int slot, bool pre) {
    rd(slot, 0, 0); SBAR();
    rd(slot, 1, 1); SBAR();
    if (pre) { stageA(kt + 1, slot ^ 1, 0); stageB(kt + 1, slot ^ 1, 0); }
    mm(0); SBAR();
    rd(slot, 2, 0); SBAR();
    if (pre) { stageA(kt + 1, slot ^ 1, 1); stageB(kt + 1, slot ^ 1, 1); }
    mm(1); SBAR();
    rd(slot, 3, 1); SBAR();
    if (pre) { stageA(kt + 1, slot ^ 1, 2); stageB(kt + 1, slot ^ 1, 2); stageA(kt + 1, slot ^ 1, 3); stageB(kt + 1, slot ^ 1, 3); }
    mm(0); SBAR();
    mm(1); SBAR();
    WAIT_VM(0); WAIT_LGKM0(); BAR(); SBAR();
  };
#pragma unroll
  for (int p = 0; p < 4; ++p) { stageA(0, 0, p); stageB(0, 0, p); }
  WAIT_VM(0); BAR(); SBAR();
  for (int kt = 0; kt < 8; kt += 2) {
    tile(kt, 0, true);
    tile(kt + 1, 1, kt + 2 < 8);
  }
#pragma unroll
  for (int a = 0; a < 2; ++a) {
    char* o = outp + ((size_t)(head * 64 + rb * 8 + wm * 2 + a) * 8) * 1024 + lane * 16;
#pragma unroll
    for (int f = 0; f < 4; ++f) {
      *(u32x4*)(o + (2 * f) * 1024) = pack8(acc[f][a], 0);
      *(u32x4*)(o + (2 * f + 1) * 1024) = pack8(acc[f][a], 8);
    }
  }
}
__global__ __launch_bounds__(512, 2) void k1_kv(const u16* __restrict__ ckv, const u16* __restrict__ wuk, const u16* __restrict__ wuv,
                                                const u16* __restrict__ x, char* __restrict__ ws) {
  extern __shared__ __attribute__((aligned(16))) char lds_[];
  LAS char* lds = (LAS char*)lds_;
  const int tid = threadIdx.x, wid = __builtin_amdgcn_readfirstlane(tid >> 6), lane = tid & 63;
  const int bid = blockIdx.x, xcd = bid & 7, idx = bid >> 3, head = 4 * xcd + (idx & 3), rb = idx >> 2;
  {
    const int row = bid * 8 + wid;
    u32x4 v0 = {0, 0, 0, 0}, v1 = {0, 0, 0, 0};
    if (lane < 36) { const u32x4* p = (const u32x4*)(x + (size_t)row * DM + lane * 16); v0 = p[0]; v1 = p[1]; }
    float f[16];
#pragma unroll
    for (int i = 0; i < 4; ++i) { f[2 * i] = bflo(v0[i]); f[2 * i + 1] = bfhi(v0[i]); f[8 + 2 * i] = bflo(v1[i]); f[8 + 2 * i + 1] = bfhi(v1[i]); }
    float s = 0.f;
#pragma unroll
    for (int i = 0; i < 16; ++i) s += f[i];
    const float mean = wave_sum(s) * (1.f / DM);
    float qv = 0.f;
#pragma unroll
    for (int i = 0; i < 16; ++i) { f[i] -= mean; qv += f[i] * f[i]; }
    if (lane >= 36) qv = 0.f;
    const float rstd = 1.f / sqrtf(wave_sum(qv) * (1.f / DM) + 1e-5f);
    if (lane < 36) {
      u32x4 o0, o1;
#pragma unroll
      for (int i = 0; i < 4; ++i) { o0[i] = cvtpk(f[2 * i] * rstd, f[2 * i + 1] * rstd); o1[i] = cvtpk(f[8 + 2 * i] * rstd, f[8 + 2 * i + 1] * rstd); }
      char* o = ws + WS_XN + ((size_t)(row >> 5) * 36 + lane) * 1024 + (row & 31) * 16;
      *(u32x4*)o = o0; *(u32x4*)(o + 512) = o1;
    }
  }
  if (wid < 4) k1_body<true>(lds, ckv, wuk, wuv, ws + WS_K, head, rb, wid, lane);
  else k1_body<false>(lds, ckv, wuk, wuv, ws + WS_V, head, rb, wid, lane);
}


constexpr int K2_LDS = 163840, K2_X = 131072;
constexpr float QSCALE = 0.08838834764831845f * 1.4426950408889634f;
constexpr float THR = 8.f;
DI void vload16(bf16x8& dst, srd_t srd, unsigned voff, unsigned soff) {
  asm volatile("buffer_load_dwordx4 %0, %1, %2, %3 offen" : "=v"(dst) : "v"(voff), "s"(srd), "s"(soff) : "memory");
}
template <int N> DI void wait_vm() { asm volatile("s_waitcnt vmcnt(%0)" ::"n"(N) : "memory"); }
#define PIN4(a) asm volatile("" : "+v"(a[0]), "+v"(a[1]), "+v"(a[2]), "+v"(a[3]))
DI float swap_max(float v) { auto r = __builtin_amdgcn_permlane32_swap(__float_as_uint(v), __float_as_uint(v), false, false); return fmaxf(__uint_as_float(r[0]), __uint_as_float(r[1])); }
DI float swap_sum(float v) { auto r = __builtin_amdgcn_permlane32_swap(__float_as_uint(v), __float_as_uint(v), false, false); return __uint_as_float(r[0]) + __uint_as_float(r[1]); }

struct AttnState { f32x16 o[4]; bf16x8 q[8]; float m, l; };

template <bool MASK>
DI void attn_step(AttnState& st, LAS const char* kp, int dq, int hi) {
  f32x16 s0 = {}, s1 = {};
#pragma unroll
  for (int sg = 0; sg < 8; ++sg) {
    s0 = MFMA32(lds_b128(kp + sg * 1024), st.q[sg], s0);
    s1 = MFMA32(lds_b128(kp + 8192 + sg * 1024), st.q[sg], s1);
  }
  if (MASK) {
    const float NEG = -__builtin_inff();
#pragma unroll
    for (int r = 0; r < 16; ++r) {
      const int key = (r & 3) + 8 * (r >> 2) + 4 * hi;
      if (key > dq) s0[r] = NEG;
      if (key + 32 > dq) s1[r] = NEG;
    }
  }
  float rm = fmaxf(s0[0], s1[0]);
#pragma unroll
  for (int r = 1; r < 16; ++r) rm = fmaxf(rm, fmaxf(s0[r], s1[r]));
  rm = swap_max(rm);
  if (__any(rm > st.m + THR)) {
    const float mn = fmaxf(st.m, rm), al = __builtin_amdgcn_exp2f(st.m - mn);
    st.m = mn; st.l *= al;
#pragma unroll
    for (int d = 0; d < 4; ++d) st.o[d] *= al;
  }
  float ls = 0.f;
#pragma unroll
  for (int r = 0; r < 16; ++r) { s0[r] = __builtin_amdgcn_exp2f(s0[r] - st.m); s1[r] = __builtin_amdgcn_exp2f(s1[r] - st.m); ls += s0[r] + s1[r]; }
  st.l += ls;
  bf16x8 p[2][2];
  p[0][0] = __builtin_bit_cast(bf16x8, pack8(s0, 0)); p[0][1] = __builtin_bit_cast(bf16x8, pack8(s0, 8));
  p[1][0] = __builtin_bit_cast(bf16x8, pack8(s1, 0)); p[1][1] = __builtin_bit_cast(bf16x8, pack8(s1, 8));
#pragma unroll
  for (int kb = 0; kb < 2; ++kb)
#pragma unroll
    for (int sp = 0; sp < 2; ++sp)
#pragma unroll
      for (int d = 0; d < 4; ++d) st.o[d] = MFMA32(lds_b128(kp + 32768 + kb * 8192 + d * 2048 + sp * 1024), p[kb][sp], st.o[d]);
}
DI void ctx_store(char* ctx, const f32x16& o, float sc, int row, int head, int db, int hi) {
  char* p = ctx + ((size_t)((row >> 4) * 128 + head * 4 + db) * 64 + hi * 16 + (row & 15)) * 16;
  f32x16 t = o * sc;
  *(u32x4*)p = pack8(t, 0);
  *(u32x4*)(p + 512) = pack8(t, 8);
}
__global__ __launch_bounds__(512, 2) void k2_attn(const u16* __restrict__ wq, char* __restrict__ ws) {
  extern __shared__ __attribute__((aligned(16))) char lds_[];
  LAS char* lds = (LAS char*)lds_;
  const unsigned ldsb = (unsigned)(size_t)lds;
  const int tid = threadIdx.x, wid = __builtin_amdgcn_readfirstlane(tid >> 6), lane = tid & 63;
  const int bid = blockIdx.x, xcd = bid & 7, idx = bid >> 3, head = 4 * xcd + (idx & 3), pi = idx >> 2;
  const int qi = wid & 3, ki = wid >> 2, r32 = lane & 31, hi = lane >> 5;
  const int blkL = pi, blkH = 15 - pi, U1 = pi + 1, U = 16 - pi;
  const srd_t sK = make_srd(ws + WS_K, 16u << 20), sV = make_srd(ws + WS_V, 16u << 20), sX = make_srd(ws + WS_XN, SEQ * DM * 2), sW = make_srd(wq, DM * NQ * 2);
  const unsigned lane16 = lane * 16;
  auto stage_kv = [&](int u) {
    const unsigned so = (unsigned)(head * 64 + 4 * u) * 8192u + wid * 4096u, lo = ldsb + (u & 1) * 65536 + wid * 4096;
#pragma unroll
    for (int p = 0; p < 4; ++p) dma16(sK, lane16, so + p * 1024, lo + p * 1024);
#pragma unroll
    for (int p = 0; p < 4; ++p) dma16(sV, lane16, so + p * 1024, lo + 32768 + p * 1024);
  };
  AttnState st;
  {
    const unsigned boff = (unsigned)(lane >> 4) * 8192u + (unsigned)(((lane & 15) ^ ((lane >> 4) << 2)) * 16);
    const unsigned xso = (unsigned)(((ki ? blkH : blkL) * 4 + qi) * 36) * 1024u;
    auto stage_w = [&](int kt) {
#pragma unroll
      for (int p = 0; p < 2; ++p) dma16(sW, boff, (unsigned)(kt * 64 + wid * 8 + p * 4) * 8192u + head * 256u, ldsb + 65536 + (kt % 6) * 16384 + wid * 2048 + p * 1024);
    };
    bf16x8 xf[4][4];
    auto load_x = [&](int kt) {
#pragma unroll
      for (int s = 0; s < 4; ++s) vload16(xf[kt & 3][s], sX, lane16, xso + (kt * 4 + s) * 1024u);
    };
    const int q = (lane & 15) >> 2, p4 = lane & 3, cb = (lane >> 4) & 1;
    const unsigned baddr = 65536u + (unsigned)(8 * hi + q) * 256u + (unsigned)(q << 6) + cb * 32 + p4 * 8;
    f32x16 acc[4];
#pragma unroll
    for (int f = 0; f < 4; ++f) acc[f] = (f32x16){};
    load_x(0); stage_w(0); load_x(1); stage_w(1); load_x(2); stage_w(2); stage_w(3); stage_w(4); stage_w(5); stage_kv(0);
    auto qtile = [&](int kt) {
      PIN4(xf[kt & 3]);
      BAR(); SBAR();
      if (kt + 3 <= 8) load_x(kt + 3);
      if (kt >= 1 && kt + 5 <= 8) stage_w(kt + 5);
      LAS const char* base = lds + (kt % 6) * 16384;
#pragma unroll
      for (int s = 0; s < 4; ++s) {
        bf16x8 wf[4];
#pragma unroll
        for (int f = 0; f < 4; ++f) { LAS const char* p = base + ((baddr ^ (f << 6)) + s * 4096); wf[f] = lds_tr2(p, p + 1024); }
#pragma unroll
        for (int f = 0; f < 4; ++f) acc[f] = MFMA32(wf[f], xf[kt & 3][s], acc[f]);
      }
      SBAR(); WAIT_LGKM0();
    };
    wait_vm<26>(); qtile(0); wait_vm<24>(); qtile(1); wait_vm<24>(); qtile(2); wait_vm<12>(); qtile(3); wait_vm<14>(); qtile(4);
    wait_vm<12>(); qtile(5); wait_vm<10>(); qtile(6); wait_vm<4>(); qtile(7); wait_vm<0>(); qtile(8);
#pragma unroll
    for (int f = 0; f < 4; ++f) {
      f32x16 t = acc[f] * QSCALE;
      st.q[2 * f] = __builtin_bit_cast(bf16x8, pack8(t, 0)); st.q[2 * f + 1] = __builtin_bit_cast(bf16x8, pack8(t, 8));
    }
    BAR(); SBAR();
    if (ki == 1) {
#pragma unroll
      for (int sg = 0; sg < 8; ++sg) *(LAS bf16x8*)(lds + K2_X + qi * 8192 + sg * 1024 + lane16) = st.q[sg];
    }
  }
#pragma unroll
  for (int d = 0; d < 4; ++d) st.o[d] = (f32x16){};
  st.m = -1e30f; st.l = 0.f;
  char* ctx = ws + WS_CTX;
  const int T1 = 2 * U1;
  for (int tau = 0; tau < 17; ++tau) {
    const bool st1 = tau < T1;
    const int u = st1 ? (tau >> 1) : (U1 + tau - T1), t = st1 ? (tau & 1) : ki;
    const bool first = !st1 || !(tau & 1), last = !st1 || (tau & 1);
    if (first && u + 1 < U) stage_kv(u + 1);
    if (tau == T1 && ki == 0) {
      const float inv = 1.f / swap_sum(st.l);
#pragma unroll
      for (int d = 0; d < 4; ++d) ctx_store(ctx, st.o[d], inv, blkL * 128 + qi * 32 + r32, head, d, hi);
#pragma unroll
      for (int d = 0; d < 4; ++d) st.o[d] = (f32x16){};
      st.m = -1e30f; st.l = 0.f;
#pragma unroll
      for (int sg = 0; sg < 8; ++sg) st.q[sg] = *(LAS const bf16x8*)(lds + K2_X + qi * 8192 + sg * 1024 + lane16);
    }
    const bool diag = st1 ? (ki == 0 && u == U1 - 1) : (u == U - 1);
    LAS const char* kp = lds + (u & 1) * 65536 + t * 16384 + lane16;
    if (!diag) attn_step<false>(st, kp, 0, hi);
    else if (t == 0 || qi >= 2) { int dq = 32 * qi + r32 - 64 * t; asm volatile("" : "+v"(dq)); attn_step<true>(st, kp, dq, hi); }
    if (last) { WAIT_VM(0); WAIT_LGKM0(); BAR(); SBAR(); }
  }
  {
    int tz = threadIdx.x; asm volatile("" : "+v"(tz));
    const int lane = tz & 63, r32 = lane & 31, hi = lane >> 5; const unsigned lane16 = lane * 16;
    const float lt = swap_sum(st.l);
    LAS f32x2* ml = (LAS f32x2*)(lds + 65536);
    if (hi == 0) ml[wid * 32 + r32] = (f32x2){st.m, lt};
    LAS char* ob = lds + wid * 8192 + lane16;
#pragma unroll
    for (int e = 0; e < 2; ++e) {
      const f32x16& t = ki ? st.o[e] : st.o[2 + e];
#pragma unroll
      for (int c = 0; c < 4; ++c) *(LAS f32x4*)(ob + (e * 4 + c) * 1024) = (f32x4){t[4 * c], t[4 * c + 1], t[4 * c + 2], t[4 * c + 3]};
    }
    WAIT_LGKM0(); BAR(); SBAR();
    const f32x2 o2 = ml[(wid ^ 4) * 32 + r32];
    const float M = fmaxf(st.m, o2[0]), a = __builtin_amdgcn_exp2f(st.m - M), b = __builtin_amdgcn_exp2f(o2[0] - M);
    const float inv = 1.f / (a * lt + b * o2[1]), sa = a * inv, sb = b * inv;
    LAS const char* pb = lds + (wid ^ 4) * 8192 + lane16;
#pragma unroll
    for (int e = 0; e < 2; ++e) {
      f32x16 t = ki ? st.o[2 + e] : st.o[e];
#pragma unroll
      for (int c = 0; c < 4; ++c) {
        const f32x4 v = *(LAS const f32x4*)(pb + (e * 4 + c) * 1024);
#pragma unroll
        for (int j = 0; j < 4; ++j) t[4 * c + j] = t[4 * c + j] * sa + v[j] * sb;
      }
      ctx_store(ctx, t, 1.f, blkH * 128 + qi * 32 + r32, head, 2 * ki + e, hi);
    }
  }
}


constexpr int RS_A = 16384, RS_SLOT = 53248, RS_LDS = 3 * RS_SLOT;
template <int ORD, int NKS  , int NST  >
__global__ __launch_bounds__(512, 2) void k_skinny(const char* __restrict__ Af, const u16* __restrict__ W, float* __restrict__ part) {
  extern __shared__ __attribute__((aligned(16))) char lds_[];
  LAS char* lds = (LAS char*)lds_;
  const unsigned ldsb = (unsigned)(size_t)lds;
  const int tid = threadIdx.x, wid = __builtin_amdgcn_readfirstlane(tid >> 6), lane = tid & 63;
  const int bid = blockIdx.x, xcd = bid & 7, i = bid >> 3, sh = xcd & 1, rowblk = (xcd >> 1) * 8 + (i >> 2), ct = i & 3;
  const srd_t sA = make_srd(Af, (unsigned)(128 * NKS) * 1024u), sW = make_srd(W, (unsigned)(NKS * 32) * DM * 2);
  const unsigned lane16 = lane * 16;
  unsigned bvo[5];
#pragma unroll
  for (int p = 0; p < 5; ++p) { const int c = (p < 4 ? wid * 4 + p : 32 + (wid & 3)) * 64 + lane, row = c / 18, cc = c - row * 18; bvo[p] = (unsigned)row * 1152u + cc * 16u; }
  const int ks0 = sh * NST * 4;
  auto stage = [&](int j, int slot) {
    const unsigned lo = ldsb + slot * RS_SLOT;
#pragma unroll
    for (int p = 0; p < 2; ++p) { const int pc = wid * 2 + p, kk = pc >> 2, m = pc & 3;
      dma16(sA, lane16, (unsigned)((rowblk * 4 + m) * NKS + ks0 + j * 4 + kk) * 1024u, lo + pc * 1024); }
    const unsigned so = (unsigned)(ks0 * 32 + j * 128) * 1152u + ct * 288u;
#pragma unroll
    for (int p = 0; p < 4; ++p) dma16(sW, bvo[p], so, lo + RS_A + (wid * 4 + p) * 1024);
    if (wid < 4) dma16(sW, bvo[4], so, lo + RS_A + (32 + wid) * 1024);
  };
  const int kg = wid & 3, mh = wid >> 2, g = lane >> 4, q = (lane & 15) >> 2, p4 = lane & 3;
  const unsigned aaddr = (unsigned)(kg * 4 + mh * 2) * 1024u + lane16;
  const int r1 = ORD == 0 ? 16 * (g >> 1) + 4 * (g & 1) : 4 * g;
  constexpr int D2 = ORD == 0 ? 8 : 16;
  const unsigned baddr = RS_A + (unsigned)(32 * kg + r1 + q) * 288u + p4 * 8;
  f32x4 acc[2][9];
#pragma unroll
  for (int m = 0; m < 2; ++m)
#pragma unroll
    for (int n = 0; n < 9; ++n) acc[m][n] = (f32x4){0.f, 0.f, 0.f, 0.f};
  stage(0, 0); stage(1, 1);
  int slot = 0;
  for (int j = 0; j < NST; ++j) {
    if (j + 1 == NST) wait_vm<0>(); else if (wid < 4) wait_vm<7>(); else wait_vm<6>();
    WAIT_LGKM0(); BAR(); SBAR();
    if (j + 2 < NST) stage(j + 2, slot == 0 ? 2 : slot - 1);
    LAS const char* base = lds + slot * RS_SLOT;
    bf16x8 xa[2], wf[9];
#pragma unroll
    for (int m = 0; m < 2; ++m) xa[m] = lds_b128(base + aaddr + m * 1024);
#pragma unroll
    for (int n = 0; n < 9; ++n) wf[n] = lds_tr2(base + baddr + n * 32, base + baddr + n * 32 + D2 * 288);
#pragma unroll
    for (int n = 0; n < 9; ++n)
#pragma unroll
      for (int m = 0; m < 2; ++m) acc[m][n] = MFMA16(wf[n], xa[m], acc[m][n]);
    slot = slot == 2 ? 0 : slot + 1;
  }
  WAIT_LGKM0(); BAR(); SBAR();
#pragma unroll
  for (int m = 0; m < 2; ++m)
#pragma unroll
    for (int n = 0; n < 9; ++n) *(LAS f32x4*)(lds + ((mh * 4 + kg) * 18 + m * 9 + n) * 1024 + lane16) = acc[m][n];
  WAIT_LGKM0(); BAR(); SBAR();
  float* po = part + (size_t)sh * SEQ * DM;
#pragma unroll
  for (int it = 0; it < 5; ++it) {
    const int e = it * 512 + tid;
    if (e < 2304) {
      const int fr = e >> 6, l = e & 63, h2 = fr / 18, jf = fr - h2 * 18, ml = jf / 9, n = jf - ml * 9;
      LAS const char* p = lds + (h2 * 72 + jf) * 1024 + l * 16;
      const f32x4 v = (*(LAS const f32x4*)p + *(LAS const f32x4*)(p + 18 * 1024)) + (*(LAS const f32x4*)(p + 36 * 1024) + *(LAS const f32x4*)(p + 54 * 1024));
      *(f32x4*)(po + (size_t)(rowblk * 64 + h2 * 32 + ml * 16 + (l & 15)) * DM + ct * 144 + n * 16 + (l >> 4) * 4) = v;
    }
  }
}

DI float bfr(float v) { return __uint_as_float(cvtpk(v, 0.f) << 16); }
__global__ __launch_bounds__(512) void k3b_ln2(const u16* __restrict__ x, char* __restrict__ ws) {
  const int tid = threadIdx.x, wid = tid >> 6, lane = tid & 63, row = blockIdx.x * 8 + wid;
  const float* p0 = (const float*)(ws + WS_P3) + (size_t)row * DM; const float* p1 = p0 + (size_t)SEQ * DM;
  float f[16];
  if (lane < 36) {
    const u32x4* px = (const u32x4*)(x + (size_t)row * DM + lane * 16);
    const u32x4 v0 = px[0], v1 = px[1];
#pragma unroll
    for (int c = 0; c < 4; ++c) {
      const f32x4 a = *(const f32x4*)(p0 + lane * 16 + c * 4), b = *(const f32x4*)(p1 + lane * 16 + c * 4);
      const unsigned w0 = c < 2 ? v0[2 * c] : v1[2 * c - 4], w1 = c < 2 ? v0[2 * c + 1] : v1[2 * c - 3];
      f[4 * c] = bfr(bflo(w0) + bfr(a[0] + b[0])); f[4 * c + 1] = bfr(bfhi(w0) + bfr(a[1] + b[1]));
      f[4 * c + 2] = bfr(bflo(w1) + bfr(a[2] + b[2])); f[4 * c + 3] = bfr(bfhi(w1) + bfr(a[3] + b[3]));
    }
  } else {
#pragma unroll
    for (int c = 0; c < 16; ++c) f[c] = 0.f;
  }
  float s = 0.f;
#pragma unroll
  for (int c = 0; c < 16; ++c) s += f[c];
  const float mean = wave_sum(s) * (1.f / DM);
  float qv = 0.f;
#pragma unroll
  for (int c = 0; c < 16; ++c) { const float d = f[c] - mean; qv += d * d; }
  if (lane >= 36) qv = 0.f;
  const float rstd = 1.f / sqrtf(wave_sum(qv) * (1.f / DM) + 1e-5f);
  if (lane < 36) {
    u32x4 h0, h1, n0, n1;
#pragma unroll
    for (int c = 0; c < 4; ++c) {
      h0[c] = cvtpk(f[2 * c], f[2 * c + 1]); h1[c] = cvtpk(f[8 + 2 * c], f[8 + 2 * c + 1]);
      n0[c] = cvtpk((f[2 * c] - mean) * rstd, (f[2 * c + 1] - mean) * rstd); n1[c] = cvtpk((f[8 + 2 * c] - mean) * rstd, (f[8 + 2 * c + 1] - mean) * rstd);
    }
    u32x4* ph = (u32x4*)(ws + WS_H + ((size_t)row * DM + lane * 16) * 2); ph[0] = h0; ph[1] = h1;
    const int kc = 2 * lane;
    char* pn = ws + WS_HN + ((size_t)((row >> 4) * 18 + (kc >> 2)) * 64 + (kc & 3) * 16 + (row & 15)) * 16;
    *(u32x4*)pn = n0; *(u32x4*)(pn + 256) = n1;
  }
}

__global__ __launch_bounds__(512, 2) void k4_up(const u16* __restrict__ W1, char* __restrict__ ws) {
  extern __shared__ __attribute__((aligned(16))) char lds_[];
  LAS char* lds = (LAS char*)lds_;
  const unsigned ldsb = (unsigned)(size_t)lds;
  const int tid = threadIdx.x, wid = __builtin_amdgcn_readfirstlane(tid >> 6), lane = tid & 63;
  const int bid = blockIdx.x, xcd = bid & 7, i = bid >> 3, rb128 = (xcd >> 1) * 4 + (i >> 3), col0 = (xcd & 1) * 2304 + (i & 7) * 288;
  const srd_t sA = make_srd(ws + WS_HN, SEQ * DM * 2), sW = make_srd(W1, DM * DFF * 2);
  const unsigned lane16 = lane * 16;
  unsigned bvo[5];
#pragma unroll
  for (int p = 0; p < 5; ++p) { const int c = (p < 4 ? wid * 4 + p : 32 + (wid & 3)) * 64 + lane, row = c / 36, cc = c - row * 36;
    bvo[p] = (unsigned)row * 9216u + (unsigned)(cc ^ (((row >> 3) & 1) << 1)) * 16u; }
  auto stage = [&](int j, int slot) {
    const unsigned lo = ldsb + slot * RS_SLOT;
#pragma unroll
    for (int p = 0; p < 2; ++p) { const int pc = wid * 2 + p, kk = pc >> 3, m = pc & 7;
      dma16(sA, lane16, (unsigned)((rb128 * 8 + m) * 18 + j * 2 + kk) * 1024u, lo + pc * 1024); }
    const unsigned so = (unsigned)(j * 64) * 9216u + col0 * 2u;
#pragma unroll
    for (int p = 0; p < 4; ++p) dma16(sW, bvo[p], so, lo + RS_A + (wid * 4 + p) * 1024);
    if (wid < 4) dma16(sW, bvo[4], so, lo + RS_A + (32 + wid) * 1024);
  };
  const int wm = wid & 3, wn = wid >> 2, g = lane >> 4, q = (lane & 15) >> 2, p4 = lane & 3;
  const unsigned aaddr = (unsigned)(wm * 2) * 1024u + lane16;
  const unsigned brow = RS_A + (unsigned)(8 * g + q) * 576u + p4 * 8 + wn * 288;
  const unsigned bA = brow + (((wn & 1) ? -1 : 1) * (g & 1)) * 32, bB = brow - (((wn & 1) ? -1 : 1) * (g & 1)) * 32;
  f32x4 acc[2][9];
#pragma unroll
  for (int m = 0; m < 2; ++m)
#pragma unroll
    for (int n = 0; n < 9; ++n) acc[m][n] = (f32x4){0.f, 0.f, 0.f, 0.f};
  stage(0, 0); stage(1, 1);
  int slot = 0;
  for (int j = 0; j < 9; ++j) {
    if (j + 1 == 9) wait_vm<0>(); else if (wid < 4) wait_vm<7>(); else wait_vm<6>();
    WAIT_LGKM0(); BAR(); SBAR();
    if (j + 2 < 9) stage(j + 2, slot == 0 ? 2 : slot - 1);
    LAS const char* base = lds + slot * RS_SLOT;
#pragma unroll
    for (int kk = 0; kk < 2; ++kk) {
      bf16x8 xa[2], wf[9];
#pragma unroll
      for (int m = 0; m < 2; ++m) xa[m] = lds_b128(base + aaddr + (kk * 8 + m) * 1024);
#pragma unroll
      for (int n = 0; n < 9; ++n) { LAS const char* p = base + ((n & 1) ? bB : bA) + n * 32 + kk * 32 * 576; wf[n] = lds_tr2(p, p + 4 * 576); }
#pragma unroll
      for (int n = 0; n < 9; ++n)
#pragma unroll
        for (int m = 0; m < 2; ++m) acc[m][n] = MFMA16(wf[n], xa[m], acc[m][n]);
    }
    slot = slot == 2 ? 0 : slot + 1;
  }
  char* h1 = ws + WS_H1;
#pragma unroll
  for (int m = 0; m < 2; ++m) {
    char* o = h1 + ((size_t)((rb128 * 8 + wm * 2 + m) * 144 + col0 / 32) * 64 + lane) * 16;
    unsigned w[9][2];
#pragma unroll
    for (int n = 0; n < 9; ++n) { w[n][0] = cvtpk(fmaxf(acc[m][n][0], 0.f), fmaxf(acc[m][n][1], 0.f)); w[n][1] = cvtpk(fmaxf(acc[m][n][2], 0.f), fmaxf(acc[m][n][3], 0.f)); }
    if (wn == 0) {
#pragma unroll
      for (int pr = 0; pr < 4; ++pr) *(u32x4*)(o + pr * 1024) = (u32x4){w[2 * pr][0], w[2 * pr][1], w[2 * pr + 1][0], w[2 * pr + 1][1]};
      *(u32x2*)(o + 4 * 1024) = (u32x2){w[8][0], w[8][1]};
    } else {
      *(u32x2*)(o + 4 * 1024 + 8) = (u32x2){w[0][0], w[0][1]};
#pragma unroll
      for (int pr = 0; pr < 4; ++pr) *(u32x4*)(o + (5 + pr) * 1024) = (u32x4){w[2 * pr + 1][0], w[2 * pr + 1][1], w[2 * pr + 2][0], w[2 * pr + 2][1]};
    }
  }
}
__global__ __launch_bounds__(256) void k5b_out(const char* __restrict__ ws, u16* __restrict__ out) {
  const int e = blockIdx.x * 256 + threadIdx.x;
  const float* p0 = (const float*)(ws + WS_P5) + (size_t)e * 8; const float* p1 = p0 + (size_t)SEQ * DM;
  const u32x4 h = *(const u32x4*)(ws + WS_H + (size_t)e * 16);
  const f32x4 a0 = *(const f32x4*)p0, a1 = *(const f32x4*)(p0 + 4), b0 = *(const f32x4*)p1, b1 = *(const f32x4*)(p1 + 4);
  u32x4 o;
  o[0] = cvtpk(bflo(h[0]) + bfr(a0[0] + b0[0]), bfhi(h[0]) + bfr(a0[1] + b0[1])); o[1] = cvtpk(bflo(h[1]) + bfr(a0[2] + b0[2]), bfhi(h[1]) + bfr(a0[3] + b0[3]));
  o[2] = cvtpk(bflo(h[2]) + bfr(a1[0] + b1[0]), bfhi(h[2]) + bfr(a1[1] + b1[1])); o[3] = cvtpk(bflo(h[3]) + bfr(a1[2] + b1[2]), bfhi(h[3]) + bfr(a1[3] + b1[3]));
  *(u32x4*)(out + (size_t)e * 8) = o;
}

extern "C" void kernel_launch(void* const* d_in, const int* in_sizes, int n_in, void* d_out, int out_size, void* d_ws, size_t ws_size,
                              hipStream_t stream) {
  const u16* x = (const u16*)d_in[0]; const u16* ckv = (const u16*)d_in[1]; const u16* wq = (const u16*)d_in[2];
  const u16* wuk = (const u16*)d_in[3]; const u16* wuv = (const u16*)d_in[4]; const u16* wo = (const u16*)d_in[5];
  const u16* w1 = (const u16*)d_in[7]; const u16* w2 = (const u16*)d_in[8];
  char* ws = (char*)d_ws;
  static bool attr = false;
  if (!attr) {
    attr = true;
    (void)hipFuncSetAttribute((const void*)k1_kv, hipFuncAttributeMaxDynamicSharedMemorySize, 2 * K1_SLOT);
    (void)hipFuncSetAttribute((const void*)k2_attn, hipFuncAttributeMaxDynamicSharedMemorySize, K2_LDS);
    (void)hipFuncSetAttribute((const void*)k_skinny<0, 128, 16>, hipFuncAttributeMaxDynamicSharedMemorySize, RS_LDS);
    (void)hipFuncSetAttribute((const void*)k_skinny<1, 144, 18>, hipFuncAttributeMaxDynamicSharedMemorySize, RS_LDS);
    (void)hipFuncSetAttribute((const void*)k4_up, hipFuncAttributeMaxDynamicSharedMemorySize, RS_LDS);
  }
  k1_kv<<<256, 512, 2 * K1_SLOT, stream>>>(ckv, wuk, wuv, x, ws);
  k2_attn<<<256, 512, K2_LDS, stream>>>(wq, ws);
  k_skinny<0, 128, 16><<<256, 512, RS_LDS, stream>>>(ws + WS_CTX, wo, (float*)(ws + WS_P3));
  k3b_ln2<<<256, 512, 0, stream>>>(x, ws);
  k4_up<<<256, 512, RS_LDS, stream>>>(w1, ws);
  k_skinny<1, 144, 18><<<256, 512, RS_LDS, stream>>>(ws + WS_H1, w2, (float*)(ws + WS_P5));
  k5b_out<<<SEQ * DM / 8 / 256, 256, 0, stream>>>(ws, (u16*)d_out);
}
```

```cpp
#include <hip/hip_runtime.h>
#include <stdint.h>
#include <utility>
#include <type_traits>
#define NOPF
#define KVAUX 16
#define RTAUX 16

#define LAS __attribute__((address_space(3)))
#define DI __device__ __forceinline__
typedef unsigned short u16;
typedef __bf16 bf16x8 __attribute__((ext_vector_type(8)));
typedef __bf16 bf16x2 __attribute__((ext_vector_type(2)));
typedef short s16x4 __attribute__((ext_vector_type(4)));
typedef short s16x8 __attribute__((ext_vector_type(8)));
typedef float f32x2 __attribute__((ext_vector_type(2)));
typedef float f32x4 __attribute__((ext_vector_type(4)));
typedef float f32x16 __attribute__((ext_vector_type(16)));
typedef unsigned u32x4 __attribute__((ext_vector_type(4)));
typedef unsigned u32x2 __attribute__((ext_vector_type(2)));

constexpr int SEQ = 2048, DM = 576, DC = 512, NH = 32, HD = 128, DFF = 4608, NQ = NH * HD;
constexpr size_t MiB = 1u << 20;
constexpr size_t WS_XN = 0, WS_K = 3 * MiB, WS_V = 19 * MiB, WS_CTX = 35 * MiB, WS_H = 51 * MiB, WS_H1 = 54 * MiB,
                 WS_WO = 72 * MiB, WS_W1 = 77 * MiB, WS_W2 = 83 * MiB,
                 WS_XB = 89 * MiB  , WS_ST = 99 * MiB  , WS_TAG = 100 * MiB  , WS_DBG = 101 * MiB, WS_END = 104 * MiB;

DI unsigned rfl(unsigned v) { return __builtin_amdgcn_readfirstlane(v); }
DI unsigned cvtpk(float lo, float hi) { f32x2 v = {lo, hi}; bf16x2 b = __builtin_convertvector(v, bf16x2); return __builtin_bit_cast(unsigned, b); }
DI float bflo(unsigned w) { return __uint_as_float(w << 16); }
DI float bfhi(unsigned w) { return __uint_as_float(w & 0xffff0000u); }
typedef __amdgpu_buffer_rsrc_t srd_t;
DI srd_t make_srd(const void* p, unsigned bytes) { return __builtin_amdgcn_make_buffer_rsrc((void*)p, (short)0, (int)bytes, 0x00020000); }
DI void dma16(srd_t srd, unsigned voff, unsigned soff, unsigned m0v) {
  asm volatile("s_mov_b32 m0, %0\n\ts_nop 0\n\tbuffer_load_dwordx4 %1, %2, %3 offen lds" ::"s"(m0v), "v"(voff), "s"(srd), "s"(soff) : "memory");
}
DI void dma16_rfl(srd_t srd, unsigned voff, unsigned soff, unsigned m0v) {
  asm volatile("s_nop 4\n\ts_mov_b32 m0, %0\n\ts_nop 0\n\tbuffer_load_dwordx4 %1, %2, %3 offen lds" ::"s"(rfl(m0v)), "v"(voff), "s"(srd), "s"(rfl(soff)) : "memory");
}
DI void dma16_sc1(srd_t srd, unsigned voff, unsigned soff, unsigned m0v) {
  asm volatile("s_mov_b32 m0, %0\n\ts_nop 0\n\tbuffer_load_dwordx4 %1, %2, %3 offen sc1 lds" ::"s"(m0v), "v"(voff), "s"(srd), "s"(soff) : "memory");
}
DI void l2_touch(srd_t srd, unsigned voff, unsigned soff, unsigned m0v) {
  asm volatile("s_mov_b32 m0, %0\n\ts_nop 0\n\tbuffer_load_dword %1, %2, %3 offen lds" ::"s"(m0v), "v"(voff), "s"(srd), "s"(soff) : "memory");
}
#define CODE_END(name) asm volatile(name "_code_end:" ::: "memory")
#define CODE_TOUCH(name, chunk, lane, m0v) do { unsigned long long pc_; unsigned len_; \
    asm volatile("s_getpc_b64 %0\n" name "_code_pc:\n\ts_mov_b32 %1, " name "_code_end-" name "_code_pc" : "=s"(pc_), "=s"(len_)); \
    const unsigned off_ = (unsigned)(chunk) * 8192u + (lane) * 128u; \
    if ((unsigned)(chunk) * 8192u < len_) { const char* p_ = (const char*)pc_ + (off_ < len_ ? off_ : 0u); \
      asm volatile("s_mov_b32 m0, %0\n\ts_nop 0\n\tglobal_load_lds_dword %1, off" :: "s"(m0v), "v"(p_) : "memory"); } } while (0)
DI bf16x8 lds_b128(LAS const char* p) { return *(LAS const bf16x8*)p; }
DI bf16x8 lds_tr2(LAS const char* p0, LAS const char* p1) {
  s16x4 a = __builtin_amdgcn_ds_read_tr16_b64_v4i16((LAS s16x4*)p0), b = __builtin_amdgcn_ds_read_tr16_b64_v4i16((LAS s16x4*)p1);
  return __builtin_bit_cast(bf16x8, (s16x8)__builtin_shufflevector(a, b, 0, 1, 2, 3, 4, 5, 6, 7));
}
#define MFMA32(a, b, c) __builtin_amdgcn_mfma_f32_32x32x16_bf16(a, b, c, 0, 0, 0)
#define MFMA16(a, b, c) __builtin_amdgcn_mfma_f32_16x16x32_bf16(a, b, c, 0, 0, 0)
DI void mfma16_acc(f32x4& c, bf16x8 a, bf16x8 b) { asm volatile("v_mfma_f32_16x16x32_bf16 %0, %1, %2, %0" : "+v"(c) : "v"(a), "v"(b)); }
#define MFMA_DRAIN() asm volatile("s_nop 7\n\ts_nop 7" ::: "memory")
#define WAIT_VM(n) asm volatile("s_waitcnt vmcnt(" #n ")" ::: "memory")
#define WAIT_LGKM0() asm volatile("s_waitcnt lgkmcnt(0)" ::: "memory")
#define BAR() __builtin_amdgcn_s_barrier()
#define STAMP_DECL() do {} while (0)
#define STAMP_PARAMS
#define STAMP_ARGS
#define STAMP(ws, kid, i) do {} while (0)
#define STAMP_DUMP(ws, kid, wave) do {} while (0)
DI int opaque_lane() { int l; asm volatile("v_mbcnt_lo_u32_b32 %0, -1, 0\n\tv_mbcnt_hi_u32_b32 %0, -1, %0" : "=v"(l)); return l; }
#define PIN(x) asm volatile("" : "+v"(x))
DI float wave_sum(float v) {
#pragma unroll
  for (int o = 32; o >= 1; o >>= 1) v += __shfl_xor(v, o);
  return v;
}
DI u32x4 pack8(const f32x16& a, int b) {
  return (u32x4){cvtpk(a[b], a[b + 1]), cvtpk(a[b + 2], a[b + 3]), cvtpk(a[b + 4], a[b + 5]), cvtpk(a[b + 6], a[b + 7])};
}

#define SBAR() __builtin_amdgcn_sched_barrier(0)
typedef __attribute__((address_space(1))) unsigned gu32;
typedef __attribute__((address_space(1))) unsigned long long gu64;
typedef unsigned long long u64;
DI void store16_sc1(srd_t srd, unsigned voff, unsigned soff, u32x4 v) { __builtin_amdgcn_raw_buffer_store_b128(v, srd, voff, soff, 16); }
DI u32x4 load16_sc1(srd_t srd, unsigned voff, unsigned soff) { return __builtin_amdgcn_raw_buffer_load_b128(srd, voff, soff, 16); }
DI void flag_set(u64* f, u64 tag) { __hip_atomic_store((gu64*)f, tag, __ATOMIC_RELAXED, __HIP_MEMORY_SCOPE_AGENT); }
DI void flags_wait(const u64* f, int n, u64 tag, int lane) {
  for (int spin = 0; spin < (1 << 16); ++spin) {
    const u64 v = lane < n ? __hip_atomic_load((gu64*)(f + lane), __ATOMIC_RELAXED, __HIP_MEMORY_SCOPE_AGENT) : tag;
    if (__all(v == tag)) break;
    __builtin_amdgcn_s_sleep(1);
  }
}
DI void flags_wait256(const u64* f, u64 tag, int lane) {
  for (int spin = 0; spin < (1 << 16); ++spin) {
    u64 v[4];
#pragma unroll
    for (int c = 0; c < 4; ++c) v[c] = __hip_atomic_load((gu64*)(f + lane * 4 + c), __ATOMIC_RELAXED, __HIP_MEMORY_SCOPE_AGENT);
    if (__all((v[0] == tag) & (v[1] == tag) & (v[2] == tag) & (v[3] == tag))) break;
    __builtin_amdgcn_s_sleep(1);
  }
}
constexpr int K1_SLOT = 65536;
template <bool KPART, class F7>
DI void kv_phase(LAS char* lds, const u16* __restrict__ ckv, const u16* __restrict__ wk, const u16* __restrict__ wv, u32x4 (&pk)[16],
                 int head, int rb, int wid, int lane, char* ws, F7&& hooks) {
  const int wm = wid & 3;
  const unsigned ldsb = (unsigned)(size_t)lds;
  unsigned aoff[2];
#pragma unroll
  for (int par = 0; par < 2; ++par) aoff[par] = (unsigned)(lane >> 3) * 1024u + (unsigned)(((lane & 7) ^ ((4 * par + (lane >> 4)) & 7)) * 16);
  const unsigned boff = (unsigned)(lane >> 4) * 8192u + (unsigned)(((lane & 15) ^ ((lane >> 4) << 2)) * 16);
  const srd_t sa = make_srd(ckv, SEQ * DC * 2), sk = make_srd(wk, DC * NQ * 2), sv = make_srd(wv, DC * NQ * 2);
  const unsigned asof = (unsigned)(rb * 256 + wm * 64) * 1024u, bsof = (unsigned)(wm * 16) * 8192u + head * 256u;
  auto stage = [&](int kt, int slot) {
    if constexpr (KPART) {
      const unsigned lo = ldsb + slot * K1_SLOT;
#pragma unroll
      for (int p = 0; p < 4; ++p) dma16(sk, boff, bsof + (unsigned)(kt * 64 + p * 4) * 8192u, lo + 32768 + wm * 4096 + p * 1024);
#pragma unroll
      for (int p = 0; p < 8; ++p) dma16(sa, aoff[p & 1], asof + p * 8192u + kt * 128u, lo + wm * 8192 + p * 1024);
#pragma unroll
      for (int p = 0; p < 4; ++p) dma16(sv, boff, bsof + (unsigned)(kt * 64 + p * 4) * 8192u, lo + 49152 + wm * 4096 + p * 1024);
    }
  };
  const int r = lane & 31, h = lane >> 5;
  const unsigned aaddr = (unsigned)(wm * 64 + r) * 128u + (unsigned)((h ^ ((r >> 1) & 7)) << 4);
  const int q = (lane & 15) >> 2, p4 = lane & 3, cb = (lane >> 4) & 1;
  const unsigned baddr = 32768u + (KPART ? 0u : 16384u) + (unsigned)(8 * h + q) * 256u + (unsigned)(q << 6) + cb * 32 + p4 * 8;
  f32x16 acc[4][2];
#pragma unroll
  for (int f = 0; f < 4; ++f)
#pragma unroll
    for (int a = 0; a < 2; ++a) acc[f][a] = (f32x16){};
  bf16x8 xa[2][2], wf[2][4];
  auto rd = [&](int slot, int s, int buf) {
    LAS const char* base = lds + slot * K1_SLOT;
#pragma unroll
    for (int a = 0; a < 2; ++a) xa[buf][a] = lds_b128(base + ((aaddr ^ (s << 5)) + a * 4096));
#pragma unroll
    for (int f = 0; f < 4; ++f) { LAS const char* p = base + ((baddr ^ (f << 6)) + s * 4096); wf[buf][f] = lds_tr2(p, p + 1024); }
  };
  auto mm = [&](int buf) {
#pragma unroll
    for (int f = 0; f < 4; ++f)
#pragma unroll
      for (int a = 0; a < 2; ++a) acc[f][a] = KPART ? MFMA32(wf[buf][f], xa[buf][a], acc[f][a]) : MFMA32(xa[buf][a], wf[buf][f], acc[f][a]);
  };
  auto tile = [&](int kt, int slot) {
    rd(slot, 0, 0); SBAR();
    rd(slot, 1, 1); SBAR();
    if (kt + 1 < 8) stage(kt + 1, slot ^ 1);
    hooks(kt);
    SBAR();
    mm(0); SBAR();
    rd(slot, 2, 0); SBAR();
    mm(1); SBAR();
    rd(slot, 3, 1); SBAR();
    mm(0); SBAR();
    mm(1); SBAR();
    if (!KPART && kt == 0) WAIT_VM(0);
    hooks(200 + kt);
    if (KPART) WAIT_VM(0);
    WAIT_LGKM0(); BAR(); SBAR();
  };
  stage(0, 0);
  hooks(-1);
  WAIT_VM(2); BAR(); SBAR();
  hooks(100);
  for (int kt = 0; kt < 6; kt += 2) { tile(kt, 0); tile(kt + 1, 1); }
  tile(6, 0);
  rd(1, 0, 0); SBAR(); rd(1, 1, 1); SBAR();
  hooks(7); SBAR();
  mm(0); SBAR(); rd(1, 2, 0); SBAR(); mm(1); SBAR(); rd(1, 3, 1); SBAR(); mm(0); SBAR(); mm(1); SBAR();
  hooks(101);
#pragma unroll
  for (int a = 0; a < 2; ++a)
#pragma unroll
    for (int f = 0; f < 4; ++f) { pk[a * 8 + 2 * f] = pack8(acc[f][a], 0); pk[a * 8 + 2 * f + 1] = pack8(acc[f][a], 8); }
}

constexpr int K2_LDS = 163840, K2_X = 131072;
constexpr float QSCALE = 0.08838834764831845f * 1.4426950408889634f;
#define THRV 24.f
constexpr float THR = THRV;
DI void vload16(bf16x8& dst, srd_t srd, unsigned voff, unsigned soff) {
  asm volatile("buffer_load_dwordx4 %0, %1, %2, %3 offen" : "=v"(dst) : "v"(voff), "s"(srd), "s"(soff) : "memory");
}
DI void vload16_sc1(bf16x8& dst, srd_t srd, unsigned voff, unsigned soff) {
  asm volatile("buffer_load_dwordx4 %0, %1, %2, %3 offen sc1" : "=v"(dst) : "v"(voff), "s"(srd), "s"(soff) : "memory");
}
template <int N> DI void wait_vm() { asm volatile("s_waitcnt vmcnt(%0)" ::"n"(N) : "memory"); }
#define PIN4(a) asm volatile("" : "+v"(a[0]), "+v"(a[1]), "+v"(a[2]), "+v"(a[3]))
DI float swap_max(float v) { auto r = __builtin_amdgcn_permlane32_swap(__float_as_uint(v), __float_as_uint(v), false, false); return fmaxf(__uint_as_float(r[0]), __uint_as_float(r[1])); }
DI float swap_sum(float v) { auto r = __builtin_amdgcn_permlane32_swap(__float_as_uint(v), __float_as_uint(v), false, false); return __uint_as_float(r[0]) + __uint_as_float(r[1]); }

#define LA 3
constexpr int NFR = LA < 4 ? 4 : 8;
#define SEG(i) do {} while (0)
struct AttnState { f32x16 o[4]; bf16x8 q[8]; f32x16 negm; u32x4 pw[4]; bf16x8 fr[NFR]; float mhat, l, f; bool resc; };
#define EXP2(x) __builtin_amdgcn_exp2f(x)
template <int N> DI void frag_read(AttnState& st, LAS const char* vp, LAS const char* kp) {
  if constexpr (N < 16) st.fr[N & (NFR - 1)] = lds_b128(vp + (N >> 3) * 8192 + (N & 3) * 2048 + ((N >> 2) & 1) * 1024);
  else st.fr[N & (NFR - 1)] = lds_b128(kp + ((N - 16) >> 3) * 8192 + ((N - 16) & 7) * 1024);
}
template <int I, bool NEXTK> DI void gapB(AttnState& st, f32x16& c0, f32x16& c1, LAS const char* vp, LAS const char* kp) {
  if constexpr (I + LA < 16 || NEXTK) frag_read<I + LA>(st, vp, kp);
  st.o[I & 3] = MFMA32(st.fr[I & (NFR - 1)], __builtin_bit_cast(bf16x8, st.pw[I >> 2]), st.o[I & 3]);
  f32x16& c = I < 8 ? c0 : c1;
  c[(2 * I) & 15] = EXP2(c[(2 * I) & 15]); c[(2 * I + 1) & 15] = EXP2(c[(2 * I + 1) & 15]);
  PIN(c);
  SBAR();
}
template <int I> DI void gapA(AttnState& st, f32x16& n0, f32x16& n1, const f32x16& p0, const f32x16& p1, float& s0, float& s1, LAS const char* kp) {
  if constexpr (I + LA < 16) frag_read<16 + I + LA>(st, kp, kp);
  f32x16& n = I < 8 ? n0 : n1;
  if constexpr ((I & 7) == 0) n = MFMA32(st.fr[I & (NFR - 1)], st.q[I & 7], st.negm);
  else n = MFMA32(st.fr[I & (NFR - 1)], st.q[I & 7], n);
  const f32x16& p = I < 8 ? p0 : p1;
  s0 += p[(2 * I) & 15]; s1 += p[(2 * I + 1) & 15];
  st.pw[I >> 2][I & 3] = cvtpk(p[(2 * I) & 15], p[(2 * I + 1) & 15]);
  PIN(s0); PIN(s1); PIN(st.pw[I >> 2]); SBAR();
}
template <bool NEXTK, int... Is> DI void phaseB_(AttnState& st, f32x16& c0, f32x16& c1, LAS const char* vp, LAS const char* kp, std::integer_sequence<int, Is...>) {
  (gapB<Is, NEXTK>(st, c0, c1, vp, kp), ...);
}
template <bool NEXTK> DI void phaseB(AttnState& st, f32x16& c0, f32x16& c1, LAS const char* vp, LAS const char* kp) {
  phaseB_<NEXTK>(st, c0, c1, vp, kp, std::make_integer_sequence<int, 16>{});
}
template <int B, int... Is> DI void frag_reads_(AttnState& st, LAS const char* vp, LAS const char* kp, std::integer_sequence<int, Is...>) { (frag_read<B + Is>(st, vp, kp), ...); }
template <int B> DI void frag_first(AttnState& st, LAS const char* vp, LAS const char* kp) { frag_reads_<B>(st, vp, kp, std::make_integer_sequence<int, LA>{}); }
template <int... Is> DI void phaseA_(AttnState& st, f32x16& n0, f32x16& n1, const f32x16& p0, const f32x16& p1, LAS const char* kp, std::integer_sequence<int, Is...>) {
  float s0 = 0.f, s1 = 0.f;
  (gapA<Is>(st, n0, n1, p0, p1, s0, s1, kp), ...);
  st.l += s0 + s1;
}
DI void phaseA(AttnState& st, f32x16& n0, f32x16& n1, const f32x16& p0, const f32x16& p1, LAS const char* kp) {
  phaseA_(st, n0, n1, p0, p1, kp, std::make_integer_sequence<int, 16>{});
}
DI void set_negm(AttnState& st) {
#pragma unroll
  for (int r = 0; r < 16; ++r) st.negm[r] = -st.mhat;
  PIN(st.negm);
}
DI void mid(AttnState& st, f32x16& c0, f32x16& c1, bool diag, int dq0, bool start) {
  if (diag) {
    const int ln = opaque_lane(), dq = dq0 + (ln & 31) - 4 * (ln >> 5);
    const float NEG = -__builtin_inff();
#pragma unroll
    for (int r = 0; r < 16; ++r) {
      const int key = (r & 3) + 8 * (r >> 2);
      if (key > dq) c0[r] = NEG;
      if (key + 32 > dq) c1[r] = NEG;
    }
  }
  float a = fmaxf(fmaxf(c0[0], c0[1]), c1[0]), b = fmaxf(fmaxf(c0[2], c0[3]), c1[1]);
  a = fmaxf(fmaxf(a, c1[2]), c1[3]);
#pragma unroll
  for (int r = 4; r < 16; r += 4) {
    a = fmaxf(fmaxf(a, c0[r]), c0[r + 1]); b = fmaxf(fmaxf(b, c0[r + 2]), c0[r + 3]);
    a = fmaxf(fmaxf(a, c1[r]), c1[r + 1]); b = fmaxf(fmaxf(b, c1[r + 2]), c1[r + 3]);
  }
  const float rm = swap_max(fmaxf(a, b));
  st.resc = false;
  if (__builtin_expect(start || __any(rm > THR), 0)) {
    const float dl = start ? rm : fmaxf(rm, 0.f);
    st.mhat += dl;
#pragma unroll
    for (int r = 0; r < 16; ++r) { c0[r] -= dl; c1[r] -= dl; }
    set_negm(st);
    if (!start) { st.f = EXP2(-dl); st.l *= st.f; st.resc = true; }
  }
  SBAR();
}
DI void ctx_store(char* ctx, const f32x16& o, float sc, int row, int head, int db, int hi) {
  const srd_t sr = make_srd(ctx, SEQ * NQ * 2);
  const unsigned p = (unsigned)(((row >> 4) * 128 + head * 4 + db) * 64 + hi * 16 + (row & 15)) * 16u;
  f32x16 t = o * sc;
  store16_sc1(sr, p, 0, pack8(t, 0));
  store16_sc1(sr, p + 512, 0, pack8(t, 8));
}
constexpr int SK_A = 8192, SK_SLOT = 26624, SK_NS = 6, BK_SIDE = SK_NS * SK_SLOT, BK_LDS = 163840;
constexpr unsigned SENT = 0xffffffffu;
constexpr int TG_H = 1024, TG_H1 = 1536;
DI float bfr(float v) { return __uint_as_float(cvtpk(v, 0.f) << 16); }
DI unsigned hfrag_off(int row, int c) { return (unsigned)((((row >> 4) * 18 + (c >> 5)) * 64 + ((c >> 3) & 3) * 16 + (row & 15)) * 16 + (c & 4) * 2); }
DI void store8_sc1(srd_t srd, unsigned voff, unsigned soff, u32x2 v) { __builtin_amdgcn_raw_buffer_store_b64(v, srd, voff, soff, 16); }

template <int NST, int N  , class FA, class FB, class FP, class FQ, class FR>
DI void produce_(FA&& stA, FB&& stB, FP&& pub, FQ&& pre, FR&& post) {
  pub();
#pragma unroll
  for (int j = 0; j < SK_NS - 1; ++j) stB(j, j);
  pre();
#pragma unroll
  for (int j = 0; j < SK_NS - 1; ++j) stA(j, j);
  post();
  wait_vm<6>(); BAR(); SBAR(); stA(5, 5); stB(5, 5);
  wait_vm<4 + N>(); BAR(); SBAR(); stA(6, 0); stB(6, 0);
  wait_vm<2 + 2 * N>(); BAR(); SBAR(); stA(7, 1); stB(7, 1);
  int slot = 2;
#pragma unroll 1
  for (int b = 3; b < NST; ++b) {
    wait_vm<3 * N>(); BAR(); SBAR();
    const int j = b + SK_NS - 1 < NST ? b + SK_NS - 1 : NST - 1; stA(j, slot); stB(j, slot);
    slot = slot == SK_NS - 1 ? 0 : slot + 1;
  }
  wait_vm<3 * N>(); BAR(); SBAR();
  wait_vm<0>();
}
template <int NST, class FA, class FB, class FP, class FQ, class FR>
DI void produce(int wid, FA&& stA, FB&& stB, FP&& pub, FQ&& pre, FR&& post) {
  if (wid < 2) produce_<NST, 7>(stA, [&](int j, int sl) { stB(j, sl, std::true_type{}); }, pub, pre, post);
  else produce_<NST, 6>(stA, [&](int j, int sl) { stB(j, sl, std::false_type{}); }, pub, pre, post);
}

struct Touch {
  srd_t sW, sA; unsigned wb, ab, wv, av, wstep, m0v; int nw, na;
  DI void w(int e) const { if (e < nw) l2_touch(sW, wv, wb + e * wstep, m0v); }
  DI void a(int e) const { if (e < na) l2_touch(sA, av, ab + e * 8192u, m0v); }
};
template <int ORD, int NST, int NB>
DI void skinny_consume(LAS char* lds, f32x4 (&acc)[2][5], int wm, int n0, int lane, const Touch& tc, int cw) {
  const unsigned aaddr = (unsigned)(wm * 2) * 1024u + lane * 16, baddr = SK_A + (unsigned)n0 * 1024u + lane * 16;
  bf16x8 xa[3][2], wf[3][NB];
  auto rdA = [&](LAS const char* base, int kk, int buf, int m) { xa[buf][m] = lds_b128(base + aaddr + (kk * 4 + m) * 1024); };
  auto rdB = [&](LAS const char* base, int kk, int buf, int n) { wf[buf][n] = lds_b128(base + baddr + (kk * 9 + n) * 1024); };
  auto kstep = [&](int cur, LAS const char* nb_, int nkk, int nbuf) {
#pragma unroll
    for (int n = 0; n < NB; ++n) {
      mfma16_acc(acc[0][n], wf[cur][n], xa[cur][0]); SBAR();
      rdB(nb_, nkk, nbuf, n); SBAR();
      mfma16_acc(acc[1][n], wf[cur][n], xa[cur][1]); SBAR();
      if (n < 2) { rdA(nb_, nkk, nbuf, n); SBAR(); }
    }
  };
  __builtin_amdgcn_s_waitcnt(0xC07F);
  BAR(); SBAR();
#pragma unroll
  for (int kk = 0; kk < 2; ++kk) {
#pragma unroll
    for (int m = 0; m < 2; ++m) rdA(lds, kk, kk, m);
#pragma unroll
    for (int n = 0; n < NB; ++n) rdB(lds, kk, kk, n);
  }
  SBAR();
  int slot = 0;
  auto stg = [&](int r) {
    const int nslot = slot == SK_NS - 1 ? 0 : slot + 1;
    LAS const char* nbase = lds + nslot * SK_SLOT;
    kstep(r % 3, nbase, 0, (r + 2) % 3);
    kstep((r + 1) % 3, nbase, 1, r % 3);
    BAR(); SBAR();
    slot = nslot;
  };
  static_assert(NST % 3 == 2 || NST % 3 == 0, "stage count");
  for (int j = 0; j + 3 <= NST; j += 3) {
    stg(0); stg(2); stg(1);
  }
  if (NST % 3 == 2) { stg(0); stg(2); }
  MFMA_DRAIN(); SBAR();
}
template <int ORD, int NST>
DI void skinny_phase_consume(LAS char* lds, char* ws, const u16* x, u16* out, int wid, int lane, int rowblk, int ct, int sh STAMP_PARAMS) {
  const int wm = (wid - 4) & 1, wn = (wid - 4) >> 1, n0 = wn * 5, nb = wn ? 4 : 5, tile = rowblk * 4 + ct;
  const unsigned lane16 = lane * 16;
  f32x4 acc[2][5];
#pragma unroll
  for (int m = 0; m < 2; ++m)
#pragma unroll
    for (int n = 0; n < 5; ++n) acc[m][n] = (f32x4){0.f, 0.f, 0.f, 0.f};
  const srd_t sH = make_srd(ws + WS_H, SEQ * DM * 2);
  const int row0 = rowblk * 64 + sh * 32 + (lane & 15), c0 = ct * 144 + n0 * 16 + (lane >> 4) * 4;
  u32x2 res[2][5];
#pragma unroll
  for (int m = 0; m < 2; ++m)
#pragma unroll
    for (int n = 0; n < 5; ++n) {
      const int nn = n < nb ? n : 0;
      res[m][n] = ORD == 0 ? *(const u32x2*)(x + (size_t)(row0 + m * 16) * DM + c0 + nn * 16) : __builtin_amdgcn_raw_buffer_load_b64(sH, hfrag_off(row0 + m * 16, c0 + nn * 16), 0, 16);
    }
  Touch tc;
  { constexpr int NKS = ORD ? 144 : 128;
    tc.sA = make_srd(ws + (ORD == 0 ? WS_CTX : WS_H1), (unsigned)(128 * NKS) * 1024u); tc.sW = make_srd(ws + (ORD == 0 ? WS_WO : WS_W2), (unsigned)(NKS * 32) * DM * 2);
    tc.wb = (unsigned)(ct * NKS + sh * NST * 2) * 9216u + (rowblk & 7) * 128u; tc.wv = lane * 1024u; tc.wstep = 65536u; tc.nw = (NST * 64 * 288 + 65535) / 65536;
    tc.ab = (unsigned)((rowblk * 4 + ct) * NKS + sh * NST * 2) * 1024u; tc.av = lane * 128u; tc.na = NST / 4;
    tc.m0v = (unsigned)(size_t)lds + BK_SIDE + 3584; }
  if (wn == 0) skinny_consume<ORD, NST, 5>(lds, acc, wm, n0, lane, tc, wid - 4);
  else skinny_consume<ORD, NST, 4>(lds, acc, wm, n0, lane, tc, wid - 4);
  STAMP(ws, 3, 1 + ORD * 6);
  const srd_t sX = make_srd(ws + WS_XB + (size_t)ORD * (256 * 20480), 256 * 20480);
  if (wm != sh) {
#pragma unroll
    for (int m = 0; m < 2; ++m)
#pragma unroll
      for (int n = 0; n < 5; ++n) if (n < nb) store16_sc1(sX, lane16, (unsigned)((tile * 2 + sh) * 20 + wn * 10 + m * 5 + n) * 1024u, __builtin_bit_cast(u32x4, acc[m][n]));
  } else {
    u32x4 pv[2][5];
    for (int spin = 0; spin < (1 << 16); ++spin) {
      bool ok = true;
      asm volatile("" ::: "memory");
#pragma unroll
      for (int m = 0; m < 2; ++m)
#pragma unroll
        for (int n = 0; n < 5; ++n) if (n < nb) pv[m][n] = load16_sc1(sX, lane16, (unsigned)((tile * 2 + (sh ^ 1)) * 20 + wn * 10 + m * 5 + n) * 1024u);
#pragma unroll
      for (int m = 0; m < 2; ++m)
#pragma unroll
        for (int n = 0; n < 5; ++n) if (n < nb) ok = ok && pv[m][n][0] != SENT && pv[m][n][1] != SENT && pv[m][n][2] != SENT && pv[m][n][3] != SENT;
      if (__all(ok)) break;
    }
    STAMP(ws, 3, 2 + ORD * 6);
#pragma unroll
    for (int m = 0; m < 2; ++m) {
      float s1 = 0.f, s2 = 0.f;
#pragma unroll
      for (int n = 0; n < 5; ++n) if (n < nb) {
        const f32x4 o = acc[m][n] + __builtin_bit_cast(f32x4, pv[m][n]);
        const float v0 = bflo(res[m][n][0]) + bfr(o[0]), v1 = bfhi(res[m][n][0]) + bfr(o[1]), v2 = bflo(res[m][n][1]) + bfr(o[2]), v3 = bfhi(res[m][n][1]) + bfr(o[3]);
        const u32x2 w = {cvtpk(v0, v1), cvtpk(v2, v3)};
        if (ORD == 0) {
          store8_sc1(sH, hfrag_off(row0 + m * 16, c0 + n * 16), 0, w);
          const float h0 = bflo(w[0]), h1 = bfhi(w[0]), h2 = bflo(w[1]), h3 = bfhi(w[1]);
          s1 += (h0 + h1) + (h2 + h3); s2 += (h0 * h0 + h1 * h1) + (h2 * h2 + h3 * h3);
        } else {
          *(u32x2*)(out + (size_t)(row0 + m * 16) * DM + c0 + n * 16) = w;
        }
      }
      if (ORD == 0) {
        s1 += __shfl_xor(s1, 16); s2 += __shfl_xor(s2, 16); s1 += __shfl_xor(s1, 32); s2 += __shfl_xor(s2, 32);
        const srd_t sS = make_srd(ws + WS_ST, SEQ * 64);
        if (lane < 16) store8_sc1(sS, (unsigned)((row0 + m * 16) * 8 + ct * 2 + wn) * 8u, 0, (u32x2){__float_as_uint(s1), __float_as_uint(s2)});
      }
    }
  }
}

template <int WM>
DI void up_consume(LAS char* lds, char* ws, int wn, int lane, int rb128, int tile) {
  const int g = lane >> 4;
  const unsigned lane16 = lane * 16, aaddr = (unsigned)(WM * 4) * 1024u + lane16, baddr = SK_A + (unsigned)wn * 9216u + lane16;
  constexpr int C0 = WM == 0 ? 0 : 5, NC = WM == 0 ? 5 : 4;
  f32x4 acc[4][9], cs[NC];
#pragma unroll
  for (int m = 0; m < 4; ++m)
#pragma unroll
    for (int n = 0; n < 9; ++n) acc[m][n] = (f32x4){0.f, 0.f, 0.f, 0.f};
#pragma unroll
  for (int n = 0; n < NC; ++n) cs[n] = (f32x4){0.f, 0.f, 0.f, 0.f};
  u32x4 ones_ = {0x3f803f80u, 0x3f803f80u, 0x3f803f80u, 0x3f803f80u}; PIN(ones_);
  const bf16x8 ones = __builtin_bit_cast(bf16x8, ones_);
  bf16x8 xa[2][4], wf[6];
  auto rdb = [&](LAS const char* base, int n, int buf) { wf[buf] = lds_b128(base + baddr + n * 1024); };
  Touch tc;
  tc.sA = make_srd(ws + WS_H, SEQ * DM * 2); tc.sW = make_srd(ws + WS_W1, DM * DFF * 2);
  tc.wb = (unsigned)tile * 331776u + (rb128 & 3) * 128u; tc.wv = lane * 512u; tc.wstep = 32768u; tc.nw = 11;
  tc.ab = (unsigned)(rb128 * 8 + (tile & 7)) * 18432u; tc.av = lane * 128u; tc.na = 3;
  tc.m0v = (unsigned)(size_t)lds + BK_SIDE + 3584;
  const int cw = wn * 2 + WM;
  __builtin_amdgcn_s_waitcnt(0xC07F);
  BAR(); SBAR();
#pragma unroll
  for (int m = 0; m < 4; ++m) xa[0][m] = lds_b128(lds + aaddr + m * 1024);
#pragma unroll
  for (int n = 0; n < 5; ++n) rdb(lds, n, n);
  SBAR();
  int slot = 0;
  auto step = [&](int cur) {
    const int nslot = slot == SK_NS - 1 ? 0 : slot + 1;
    LAS const char* base = lds + slot * SK_SLOT;
    LAS const char* nbase = lds + nslot * SK_SLOT;
#pragma unroll
    for (int n = 0; n < 9; ++n) {
      const int t = cur * 9 + n;
      const bf16x8 w = wf[t % 6];
      mfma16_acc(acc[0][n], w, xa[cur][0]); SBAR();
      if (n + 5 < 9) rdb(base, n + 5, (t + 5) % 6); else rdb(nbase, n + 5 - 9, (t + 5) % 6);
      SBAR();
      mfma16_acc(acc[1][n], w, xa[cur][1]); SBAR();
      mfma16_acc(acc[2][n], w, xa[cur][2]); SBAR();
      if (n >= 1 && n <= 4) { xa[cur ^ 1][n - 1] = lds_b128(nbase + aaddr + (n - 1) * 1024); SBAR(); }
      mfma16_acc(acc[3][n], w, xa[cur][3]); SBAR();
      if (n >= C0 && n < C0 + NC) { mfma16_acc(cs[n - C0], w, ones); SBAR(); }
    }
    BAR(); SBAR();
    slot = nslot;
  };
#pragma unroll 1
  for (int j = 0; j < 18; j += 2) {
    step(0); step(1);
  }
  MFMA_DRAIN(); SBAR();
  LAS f32x4* csx = (LAS f32x4*)(lds + BK_SIDE) + wn * 36;
  if ((lane & 15) == 0) {
#pragma unroll
    for (int n = 0; n < NC; ++n) csx[(C0 + n) * 4 + g] = cs[n];
  }
  WAIT_LGKM0(); BAR(); SBAR();
  const srd_t sO = make_srd(ws + WS_H1, SEQ * DFF * 2);
  LAS const f32x2* stash = (LAS const f32x2*)(lds + BK_SIDE + 1536);
  f32x4 cv[9];
#pragma unroll
  for (int n = 0; n < 9; ++n) cv[n] = csx[n * 4 + g];
#pragma unroll
  for (int m = 0; m < 4; ++m) {
    const unsigned o = (unsigned)((rb128 * 8 + WM * 4 + m) * 144 + tile * 9) * 1024u;
    const f32x2 mr = stash[(WM * 4 + m) * 16 + (lane & 15)];
    unsigned w[9][2];
#pragma unroll
    for (int n = 0; n < 9; ++n) {
      const f32x4 u = (acc[m][n] - cv[n] * mr[0]) * mr[1];
      w[n][0] = cvtpk(fmaxf(u[0], 0.f), fmaxf(u[1], 0.f)); w[n][1] = cvtpk(fmaxf(u[2], 0.f), fmaxf(u[3], 0.f));
    }
    if (wn == 0) {
#pragma unroll
      for (int pr = 0; pr < 4; ++pr) store16_sc1(sO, lane16, o + pr * 1024, (u32x4){w[2 * pr][0], w[2 * pr][1], w[2 * pr + 1][0], w[2 * pr + 1][1]});
      store8_sc1(sO, lane16, o + 4 * 1024, (u32x2){w[8][0], w[8][1]});
    } else {
      store8_sc1(sO, lane16 + 8, o + 4 * 1024, (u32x2){w[0][0], w[0][1]});
#pragma unroll
      for (int pr = 0; pr < 4; ++pr) store16_sc1(sO, lane16, o + (5 + pr) * 1024, (u32x4){w[2 * pr + 1][0], w[2 * pr + 1][1], w[2 * pr + 2][0], w[2 * pr + 2][1]});
    }
  }
}

constexpr int TG_CTX = 2048, TG_AUX = 2560;
DI void back_phases(LAS char* lds, const u16* __restrict__ x, char* __restrict__ ws, u16* __restrict__ out, u64 tag STAMP_PARAMS) {
  const unsigned ldsb = (unsigned)(size_t)lds;
  const int tid = threadIdx.x, wid = __builtin_amdgcn_readfirstlane(tid >> 6), lane = opaque_lane();
  const int bid = blockIdx.x, xcd = bid & 7, i = bid >> 3, sh = xcd & 1;
  const int rowblk = (xcd >> 1) * 8 + (i >> 2), ct = i & 3;
  const int rb128 = (xcd >> 1) * 4 + (i >> 3), tile = sh * 8 + (i & 7);
  const unsigned lane16 = lane * 16;
  u64* tg = (u64*)(ws + WS_TAG);
  if (wid < 4) {
    const int b0 = wid < 2 ? 5 * wid : 2 + 4 * wid;
    auto skinny = [&](auto ordc, auto&& pub, auto&& pre) {
      constexpr int ORD = decltype(ordc)::value, NKS = ORD ? 144 : 128, NST = ORD ? 36 : 32;
      const srd_t sA = make_srd(ws + (ORD == 0 ? WS_CTX : WS_H1), (unsigned)(128 * NKS) * 1024u), sW = make_srd(ws + (ORD == 0 ? WS_WO : WS_W2), (unsigned)(NKS * 32) * DM * 2);
      const int ks0 = sh * NST * 2;
      auto stA = [&](int j, int slot) {
#pragma unroll
        for (int p = 0; p < 2; ++p) { const int pc = 2 * wid + p;
          dma16_sc1(sA, lane16, (unsigned)((rowblk * 4 + (pc & 3)) * NKS + ks0 + j * 2 + (pc >> 2)) * 1024u, ldsb + slot * SK_SLOT + pc * 1024); }
      };
      auto stB = [&](int j, int slot, auto five) {
        const unsigned so = (unsigned)((ct * NKS + ks0 + j * 2) * 9 + b0) * 1024u, lo = ldsb + slot * SK_SLOT + SK_A + b0 * 1024;
#pragma unroll
        for (int p = 0; p < 4; ++p) dma16(sW, lane16, so + p * 1024, lo + p * 1024);
        if constexpr (decltype(five)::value) dma16(sW, lane16, so + 4096, lo + 4096);
      };
      produce<NST>(wid, stA, stB, pub, pre, [&]() {});
    };
    skinny(std::integral_constant<int, 0>{}, [&]() { BAR();   }, [&]() { BAR();   });
    STAMP(ws, 0, 11);
    {
      const srd_t sA = make_srd(ws + WS_H, SEQ * DM * 2), sW = make_srd(ws + WS_W1, DM * DFF * 2), sS = make_srd(ws + WS_ST, SEQ * 64);
      auto stA = [&](int j, int slot) {
#pragma unroll
        for (int p = 0; p < 2; ++p) dma16_sc1(sA, lane16, (unsigned)((rb128 * 8 + 2 * wid + p) * 18 + j) * 1024u, ldsb + slot * SK_SLOT + (2 * wid + p) * 1024);
      };
      auto stB = [&](int j, int slot, auto five) {
        const unsigned so = (unsigned)((tile * 18 + j) * 18 + b0) * 1024u, lo = ldsb + slot * SK_SLOT + SK_A + b0 * 1024;
#pragma unroll
        for (int p = 0; p < 4; ++p) dma16(sW, lane16, so + p * 1024, lo + p * 1024);
        if constexpr (decltype(five)::value) dma16(sW, lane16, so + 4096, lo + 4096);
      };
      produce<18>(wid, stA, stB, [&]() {
        BAR();
        if (tid == 0) flag_set(tg + TG_H + (rowblk >> 1) * 16 + (rowblk & 1) * 8 + ct * 2 + sh, tag);
      }, [&]() {
        BAR();
        STAMP(ws, 0, 12);
      }, [&]() {
        bf16x8 sv[4];
#pragma unroll
        for (int c = 0; c < 4; ++c) vload16_sc1(sv[c], sS, (unsigned)(lane & 31) * 64u + c * 16u, (unsigned)(rb128 * 128 + wid * 32) * 64u);
        wait_vm<0>();
        asm volatile("" : "+v"(sv[0]), "+v"(sv[1]), "+v"(sv[2]), "+v"(sv[3]));
        float s1 = 0.f, s2 = 0.f;
#pragma unroll
        for (int c = 0; c < 4; ++c) { const f32x4 a = __builtin_bit_cast(f32x4, sv[c]); s1 += a[0] + a[2]; s2 += a[1] + a[3]; }
        const float mu = s1 * (1.f / DM);
        if (lane < 32) ((LAS f32x2*)(lds + BK_SIDE + 1536))[wid * 32 + lane] = (f32x2){mu, 1.f / sqrtf(fmaxf(s2 * (1.f / DM) - mu * mu, 0.f) + 1e-5f)};
        WAIT_LGKM0();
      });
      BAR();
    }
    STAMP(ws, 0, 13);
    skinny(std::integral_constant<int, 1>{}, [&]() {
      BAR();
      if (tid == 0) flag_set(tg + TG_H1 + rb128 * 16 + tile, tag);
    }, [&]() {
      BAR();
      STAMP(ws, 0, 14);
    });
    STAMP(ws, 0, 15);
  } else {
    if (wid == 5) flags_wait256(tg + TG_AUX, tag, lane);
    BAR();
    if (wid == 4) flags_wait(tg + TG_CTX + (rowblk >> 1) * 32 + sh * 16, 16, tag, lane);
    BAR();
    skinny_phase_consume<0, 32>(lds, ws, x, out, wid, lane, rowblk, ct, sh STAMP_ARGS);
    STAMP(ws, 3, 3);
    asm volatile("s_waitcnt vmcnt(0)" ::: "memory");
    STAMP(ws, 3, 4);
    BAR();
    if (wid == 4) flags_wait(tg + TG_H + rb128 * 16, 16, tag, lane);
    BAR();
    STAMP(ws, 3, 5);
    if (((wid - 4) & 1) == 0) up_consume<0>(lds, ws, (wid - 4) >> 1, lane, rb128, tile);
    else up_consume<1>(lds, ws, (wid - 4) >> 1, lane, rb128, tile);
    STAMP(ws, 3, 6);
    asm volatile("s_waitcnt vmcnt(0)" ::: "memory");
    BAR();
    if (wid == 4) flags_wait(tg + TG_H1 + (rowblk >> 1) * 16 + sh * 8, 8, tag, lane);
    BAR();
    skinny_phase_consume<1, 36>(lds, ws, x, out, wid, lane, rowblk, ct, sh STAMP_ARGS);
    STAMP(ws, 3, 9);
  }
  STAMP(ws, 0, 16);
}

constexpr int RT_U0 = 768, RT_U1 = 768 + 864, RT_UN = 768 + 864 + 864;
DI void retile_issue(int u, int lane, unsigned ldsw, const u16* wo, const u16* w2, const u16* w1) {
  const int which = u < RT_U0 ? 0 : u < RT_U1 ? 1 : 2, v = u - (which == 0 ? 0 : which == 1 ? RT_U0 : RT_U1);
  const int kb = which == 2 ? v / 48 : v / 6, cg = v - kb * (which == 2 ? 48 : 6);
  const unsigned stride = which == 2 ? 9216u : 1152u;
  const srd_t sr = make_srd(which == 0 ? wo : which == 1 ? w2 : w1, which == 0 ? 4096u * 1152u : 4608u * 1152u);
#pragma unroll
  for (int p = 0; p < 6; ++p) {
    const int c = p * 64 + lane, row = (c * 5462) >> 16  , cc = c - row * 12;
    dma16_rfl(sr, (unsigned)row * stride + cc * 16u, (unsigned)(kb * 32) * stride + cg * 192u, ldsw + p * 1024);
  }
}
DI void retile_finish(int u, int lane, LAS const char* img, char* ws) {
  const int which = u < RT_U0 ? 0 : u < RT_U1 ? 1 : 2, v = u - (which == 0 ? 0 : which == 1 ? RT_U0 : RT_U1);
  const int kb = which == 2 ? v / 48 : v / 6, cg = v - kb * (which == 2 ? 48 : 6);
  const int g = lane >> 4, q = (lane & 15) >> 2, p4 = lane & 3;
  const int r1 = which == 0 ? 16 * (g >> 1) + 4 * (g & 1) : which == 1 ? 4 * g : 8 * g, d2 = which == 0 ? 8 : which == 1 ? 16 : 4;
  LAS const char* p = img + (r1 + q) * 192 + p4 * 8;
  bf16x8 fr[6];
#pragma unroll
  for (int f = 0; f < 6; ++f) fr[f] = lds_tr2(p + f * 32, p + f * 32 + d2 * 192);
  const srd_t sr = make_srd(ws + (which == 0 ? WS_WO : which == 1 ? WS_W2 : WS_W1), which == 0 ? 4096u * 1152u : 4608u * 1152u);
  const int perf = which == 2 ? 18 : 9, nf0 = cg * 6, t0 = which == 2 ? (nf0 * 57) >> 10 : (nf0 * 114) >> 10, n0 = nf0 - t0 * perf;
  const int tstep = which == 0 ? 128 * 9 : which == 1 ? 144 * 9 : 18 * 18, o0 = t0 * tstep + kb * perf + n0, wrap = perf - n0;
#pragma unroll
  for (int f = 0; f < 6; ++f)
    __builtin_amdgcn_raw_buffer_store_b128(__builtin_bit_cast(u32x4, fr[f]), sr, lane * 16, (unsigned)(o0 + f + (f >= wrap ? tstep - perf : 0)) * 1024u, RTAUX);
}
constexpr int TG_XN = 0, TG_KV = 256, TG_EP = 512;
__global__ __launch_bounds__(512, 2) void k_all(const u16* __restrict__ x, const u16* __restrict__ ckv, const u16* __restrict__ wq, const u16* __restrict__ wuk,
                                                const u16* __restrict__ wuv, const u16* __restrict__ wo, const u16* __restrict__ w1, const u16* __restrict__ w2, char* __restrict__ ws, u16* __restrict__ out) {
  extern __shared__ __attribute__((aligned(16))) char lds_[];
  LAS char* lds = (LAS char*)lds_;
  const unsigned ldsb = (unsigned)(size_t)lds;
  const int tid = threadIdx.x, wid = __builtin_amdgcn_readfirstlane(tid >> 6), lane = tid & 63;
  const int bid = blockIdx.x, xcd = bid & 7, idx = bid >> 3, head = 4 * xcd + (idx & 3), pi = idx >> 2;
  const int qi = wid & 3, ki = wid >> 2, hi = lane >> 5;
  const int blkL = pi, blkH = 15 - pi, U1 = pi + 1;
  const unsigned lane16 = lane * 16;
  u64* tg = (u64*)(ws + WS_TAG);
  STAMP_DECL();
  STAMP(ws, 0, 0);
  u64 tag;
  { const u64 e = __hip_atomic_load((gu64*)(tg + TG_EP), __ATOMIC_RELAXED, __HIP_MEMORY_SCOPE_AGENT) + 1;
    tag = ((u64)(unsigned)__builtin_amdgcn_readfirstlane((unsigned)(e >> 32)) << 32) | (unsigned)__builtin_amdgcn_readfirstlane((unsigned)e); }
  if (wid == 7) CODE_TOUCH(".Lall", idx, lane, ldsb + 131072 + 24576 + 4096);
  const srd_t sW = make_srd(wq, DM * NQ * 2);
  const unsigned boff = (unsigned)(lane >> 4) * 8192u + (unsigned)(((lane & 15) ^ ((lane >> 4) << 2)) * 16);
  auto wslot = [&](int kt) { const int j = kt % 6; return (unsigned)(j < 4 ? j * 16384 : 131072 + (j - 4) * 16384); };
  auto stage_w = [&](int kt) {
#pragma unroll
    for (int p = 0; p < 2; ++p) dma16_rfl(sW, boff, (unsigned)(kt * 64 + wid * 8 + p * 4) * 8192u + head * 256u, ldsb + wslot(kt) + wid * 2048 + p * 1024);
  };
  u32x4 pk[16];
  u32x4 fl0 = {0, 0, 0, 0}, fl1 = {0, 0, 0, 0};
  auto hooks = [&](int kt) {
    if (kt == 100) STAMP(ws, 0, 1);
    if (kt == 101) STAMP(ws, 0, 2);
    if (kt == -1) {
      const int row = bid * 8 + wid;
      u32x4 v0 = {0, 0, 0, 0}, v1 = {0, 0, 0, 0};
      if (lane < 36) { const u32x4* p = (const u32x4*)(x + (size_t)row * DM + lane * 16); v0 = p[0]; v1 = p[1]; }
      float f[16];
#pragma unroll
      for (int i = 0; i < 4; ++i) { f[2 * i] = bflo(v0[i]); f[2 * i + 1] = bfhi(v0[i]); f[8 + 2 * i] = bflo(v1[i]); f[8 + 2 * i + 1] = bfhi(v1[i]); }
      float sm = 0.f;
#pragma unroll
      for (int i = 0; i < 16; ++i) sm += f[i];
      const float mean = wave_sum(sm) * (1.f / DM);
      float qv = 0.f;
#pragma unroll
      for (int i = 0; i < 16; ++i) { f[i] -= mean; qv += f[i] * f[i]; }
      if (lane >= 36) qv = 0.f;
      const float rstd = 1.f / sqrtf(wave_sum(qv) * (1.f / DM) + 1e-5f);
      if (lane < 36) {
        u32x4 o0, o1;
#pragma unroll
        for (int i = 0; i < 4; ++i) { o0[i] = cvtpk(f[2 * i] * rstd, f[2 * i + 1] * rstd); o1[i] = cvtpk(f[8 + 2 * i] * rstd, f[8 + 2 * i + 1] * rstd); }
        const srd_t sX = make_srd(ws + WS_XN, SEQ * DM * 2);
        const unsigned o = (unsigned)((row >> 5) * 36 + lane) * 1024u + (row & 31) * 16;
        store16_sc1(sX, o, 0, o0); store16_sc1(sX, o + 512, 0, o1);
      }
    } else if (kt == 1) {
      if (tid == 0) flag_set(tg + TG_XN + bid, tag);
    } else if (kt == 2) {
      if (wid == 0) {
        const u64* p = tg + TG_XN + lane * 4;
        asm volatile("global_load_dwordx4 %0, %2, off sc1\n\tglobal_load_dwordx4 %1, %2, off offset:16 sc1" : "=&v"(fl0), "=&v"(fl1) : "v"(p) : "memory");
      }
    } else if (kt == 3) {
      if (wid == 0) {
        asm volatile("" : "+v"(fl0), "+v"(fl1));
        const unsigned tl = (unsigned)tag, th = (unsigned)(tag >> 32);
        const bool ok = fl0[0] == tl && fl0[1] == th && fl0[2] == tl && fl0[3] == th && fl1[0] == tl && fl1[1] == th && fl1[2] == tl && fl1[3] == th;
        if (!__all(ok)) flags_wait256(tg + TG_XN, tag, lane);
        if (bid == 0 && lane == 0) flag_set(tg + TG_EP, tag);
      }
    } else if (kt == 7) {
#pragma unroll
      for (int j = 0; j < 6; ++j) stage_w(j);
    } else if (kt >= 200) {
      if (wid >= 4) {
        const int step = kt - 200, ln = opaque_lane(), tz = ln;
        const unsigned imgo = 131072 + (wid - 4) * 6144;
        if ((step & 1) == 0) {
          const int r = step >> 1;
          if (r >= 1) {
            const int u = ((r - 1) * 256 + bid) * 4 + (wid - 4);
            if (u < RT_UN) { WAIT_VM(0); retile_finish(u, ln, lds + imgo, ws); WAIT_LGKM0(); }
          }
          if (r <= 2) {
            const int u = (r * 256 + bid) * 4 + (wid - 4);
            if (u < RT_UN) retile_issue(u, ln, ldsb + imgo, wo, w2, w1);
          }
        }
        const unsigned dm = ldsb + 131072 + 24576 + (wid - 4) * 1024;
        if (step == 1) {
          const int c = wid - 4;
#pragma unroll
          for (int p = 0; p < 5; ++p) { const int pc = c * 5 + p; if (pc < 18) dma16_rfl(sW, (unsigned)(ln >> 4) * 8192u + (ln & 15) * 16u, (unsigned)(pi * 72 + pc * 4) * 8192u + head * 256u, dm); }
        }
      }
    }
  };
  if (wid < 4) kv_phase<true>(lds, ckv, wuk, wuv, pk, head, pi, wid, lane, ws, hooks);
  else kv_phase<false>(lds, ckv, wuk, wuv, pk, head, pi, wid, lane, ws, hooks);
  const srd_t sK = make_srd(ws + WS_K, 16u << 20), sV = make_srd(ws + WS_V, 16u << 20);
  AttnState st;
  STAMP(ws, 0, 3);
  {
    const srd_t sX = make_srd(ws + WS_XN, SEQ * DM * 2), sO = ki ? sV : sK;
    const unsigned xso = (unsigned)(((ki ? blkH : blkL) * 4 + qi) * 36) * 1024u;
    const unsigned oso = (unsigned)(head * 64 + pi * 8 + qi * 2) * 8192u;
    bf16x8 xf[4][4];
    auto load_x = [&](int kt) {
#pragma unroll
      for (int s = 0; s < 4; ++s) vload16_sc1(xf[kt & 3][s], sX, lane16, xso + (kt * 4 + s) * 1024u);
    };
    const int q = (lane & 15) >> 2, p4 = lane & 3, cb = (lane >> 4) & 1;
    const unsigned baddr = (unsigned)(8 * hi + q) * 256u + (unsigned)(q << 6) + cb * 32 + p4 * 8;
    f32x16 acc[4];
#pragma unroll
    for (int f = 0; f < 4; ++f) acc[f] = (f32x16){};
    load_x(0); load_x(1); load_x(2);
    bf16x8 wf[4];
    unsigned bad[4];
#pragma unroll
    for (int f = 0; f < 4; ++f) bad[f] = baddr ^ (f << 6);
    auto rdw = [&](int n) { const int kt = n >> 4, s_ = (n >> 2) & 3, f = n & 3; LAS const char* p = lds + wslot(kt) + s_ * 4096 + bad[f]; wf[n & 3] = lds_tr2(p, p + 1024); };
    auto qtile = [&](int kt) {
      PIN4(xf[kt & 3]);
      BAR(); SBAR();
      if (kt == 0) { rdw(0); rdw(1); rdw(2); }
      if (kt + 3 <= 8) load_x(kt + 3);
      if (kt >= 1 && kt + 5 <= 8) stage_w(kt + 5);
      if (kt < 4) {
#pragma unroll
        for (int j = 0; j < 4; ++j) __builtin_amdgcn_raw_buffer_store_b128(pk[kt * 4 + j], sO, lane16, oso + (kt * 4 + j) * 1024u, KVAUX);
      }
      if (kt == 7 && tid == 0) flag_set(tg + TG_KV + head * 8 + pi, tag);
      SBAR();
#pragma unroll
      for (int i = 0; i < 16; ++i) {
        const int n = kt * 16 + i;
        if (n + 3 < 144) rdw(n + 3);
        acc[i & 3] = MFMA32(wf[n & 3], xf[kt & 3][i >> 2], acc[i & 3]);
        SBAR();
      }
    };
    wait_vm<8>(); STAMP(ws, 0, 4); qtile(0); wait_vm<12>(); qtile(1); wait_vm<18>(); qtile(2); wait_vm<24>(); qtile(3); wait_vm<26>(); qtile(4);
    wait_vm<20>(); qtile(5); wait_vm<14>(); qtile(6); wait_vm<4>(); qtile(7); wait_vm<0>(); qtile(8);
    STAMP(ws, 0, 5);
    if (wid == 0) flags_wait(tg + TG_KV + head * 8, 8, tag, lane);
    STAMP(ws, 0, 6);
#pragma unroll
    for (int f = 0; f < 4; ++f) {
      f32x16 t = acc[f] * QSCALE;
      st.q[2 * f] = __builtin_bit_cast(bf16x8, pack8(t, 0)); st.q[2 * f + 1] = __builtin_bit_cast(bf16x8, pack8(t, 8));
    }
    BAR(); SBAR();
    { const unsigned so = (unsigned)(head * 64) * 8192u + wid * 4096u, lo = ldsb + 65536 + wid * 4096;
#pragma unroll
      for (int p = 0; p < 4; ++p) dma16_sc1(sK, lane16, so + p * 1024, lo + p * 1024);
#pragma unroll
      for (int p = 0; p < 4; ++p) dma16_sc1(sV, lane16, so + p * 1024, lo + 32768 + p * 1024); }
    if (ki == 1) {
#pragma unroll
      for (int sg = 0; sg < 8; ++sg) *(LAS bf16x8*)(lds + K2_X + qi * 8192 + sg * 1024 + lane16) = st.q[sg];
    }
    WAIT_VM(0); WAIT_LGKM0(); BAR(); SBAR();
  }
#pragma unroll
  for (int d = 0; d < 4; ++d) st.o[d] = (f32x16){};
  st.mhat = 0.f; st.l = 0.f; st.f = 1.f; st.resc = false;
  set_negm(st);
  char* ctx = ws + WS_CTX;
  STAMP(ws, 0, 7);
  const int T1 = 2 * U1;
  auto stg = [&](int tau) { const int a_ = tau >> 1, b_ = tau - U1; return a_ > b_ ? a_ : b_; };
  auto toff = [&](int tau) { return (unsigned)(((stg(tau) + 1) & 1) * 65536 + (tau < T1 ? (tau & 1) : ki) * 16384); };
  f32x16 sa0, sa1, sb0, sb1;
  LAS const char* lb = lds + lane16;
  const unsigned so0 = (unsigned)(head * 64) * 8192u + qi * 8192u, lo0 = ldsb + qi * 8192;
  bool doK = false, doV = false; int uK = 0, uV = 0;
  auto plan = [&](int tau) {
    uK = stg(tau + 2); uV = stg(tau);
    doK = (tau + 2 <= 16) & (uK != stg(tau + 1)); doV = (tau > 0) & (uV != stg(tau - 1));
  };
  unsigned tP = toff(0), tN = toff(1);
  auto run = [&](auto g1c) {
    constexpr bool G1 = decltype(g1c)::value;
    {
      LAS const char* kp = lb + toff(0);
      frag_first<16>(st, kp, kp);
      sb0 = (f32x16){}; sb1 = (f32x16){};
      phaseA(st, sa0, sa1, sb0, sb1, kp);
      plan(0);
      if constexpr (G1) mid(st, sa0, sa1, false, 0, true);
      WAIT_LGKM0(); BAR(); SBAR();
    }
    auto period = [&](int tau, f32x16& c0, f32x16& c1, f32x16& n0, f32x16& n1, auto lastc) {
      constexpr bool LAST = decltype(lastc)::value;
      LAS const char* vp = lb + 32768 + tP;
      LAS const char* kp = lb + tN;
      frag_first<0>(st, vp, kp);
      if constexpr (!G1) {
        if (tau == T1 + 1 && tid == 0) flag_set(tg + TG_CTX + blkL * 32 + head, tag);
        if (tau == 12 && tid == 0) flag_set(tg + TG_AUX + bid, tag);
      }
      if constexpr (!G1) {
        if (doK) {
#pragma unroll
          for (int p = 0; p < 8; ++p) dma16_sc1(sK, lane16, so0 + uK * 32768u + p * 1024, lo0 + ((uK + 1) & 1) * 65536 + p * 1024);
        }
        if (doV) {
#pragma unroll
          for (int p = 0; p < 8; ++p) dma16_sc1(sV, lane16, so0 + uV * 32768u + p * 1024, lo0 + ((uV + 1) & 1) * 65536 + 32768 + p * 1024);
        }
      }
      SEG(0);
      if constexpr (!G1) {
        const bool diag = LAST || (tau >= T1 - 2 && tau < T1);
        mid(st, c0, c1, diag, 32 * qi - (tau < T1 ? 64 * (tau & 1) : 0), tau == 0 || tau == T1);
      }
      SEG(1);
      phaseB<!LAST>(st, c0, c1, vp, kp);
      SEG(2);
      if (st.resc) {
#pragma unroll
        for (int d = 0; d < 4; ++d) st.o[d] *= st.f;
      }
      if constexpr (!G1) {
        if (tau == T1) {
          const float inv = 1.f / swap_sum(st.l);
          const int ln = opaque_lane();
#pragma unroll
          for (int d = 0; d < 4; ++d) ctx_store(ctx, st.o[d], inv, blkL * 128 + qi * 32 + (ln & 31), head, d, ln >> 5);
#pragma unroll
          for (int d = 0; d < 4; ++d) st.o[d] = (f32x16){};
          st.l = 0.f;
        }
      }
      if constexpr (!LAST) {
        if constexpr (!G1) {
          if (tau == T1 - 1) {
#pragma unroll
            for (int sg = 0; sg < 8; ++sg) st.q[sg] = *(LAS const bf16x8*)(lb + K2_X + qi * 8192 + sg * 1024);
            st.mhat = 0.f; set_negm(st);
          }
        }
        tP = toff(tau); tN = toff(tau + 2); plan(tau + 1);
        SEG(3);
        phaseA(st, n0, n1, c0, c1, kp);
        if constexpr (G1) mid(st, n0, n1, tau == 15, 32 * qi - 64, false);
      }
      if constexpr (G1 && !LAST) {
        if (tau < 10) {
          const srd_t sr = make_srd(ws + WS_XB, 2 * 256 * 20480);
          const int f = tau * 4 + qi;
          __builtin_amdgcn_raw_buffer_store_b128((u32x4){0xffffffffu, 0xffffffffu, 0xffffffffu, 0xffffffffu}, sr, lane16, (unsigned)((f / 20) * 256 * 20 + bid * 20 + f % 20) * 1024u, RTAUX);
        }
      }
      SEG(4);
      if constexpr (!G1) WAIT_VM(0); else { if (tau == 11) WAIT_VM(0); }
      WAIT_LGKM0();
      SEG(5);
      BAR();
      SBAR();
      SEG(6);
    };
    for (int tau = 0; tau < 16; tau += 2) {
      period(tau, sa0, sa1, sb0, sb1, std::false_type{});
      period(tau + 1, sb0, sb1, sa0, sa1, std::false_type{});
    }
    period(16, sa0, sa1, sb0, sb1, std::true_type{});
  };
  if (ki == 0) run(std::false_type{}); else run(std::true_type{});
  {
    LAS const char* vp = lb + 32768 + toff(16);
    float ls = 0.f;
#pragma unroll
    for (int r = 0; r < 16; ++r) ls += sa0[r] + sa1[r];
    st.l += ls;
    st.pw[0] = pack8(sa0, 0); st.pw[1] = pack8(sa0, 8); st.pw[2] = pack8(sa1, 0); st.pw[3] = pack8(sa1, 8);
#pragma unroll
    for (int i = 0; i < 16; ++i)
      st.o[i & 3] = MFMA32(lds_b128(vp + (i >> 3) * 8192 + (i & 3) * 2048 + ((i >> 2) & 1) * 1024), __builtin_bit_cast(bf16x8, st.pw[i >> 2]), st.o[i & 3]);
    WAIT_LGKM0(); BAR(); SBAR();
  }
  STAMP(ws, 0, 8);
  {
    const int lane = opaque_lane(), r32 = lane & 31, hi = lane >> 5; const unsigned lane16 = lane * 16;
    const float lt = swap_sum(st.l);
    LAS f32x2* ml = (LAS f32x2*)(lds + 65536);
    if (hi == 0) ml[wid * 32 + r32] = (f32x2){st.mhat, lt};
    LAS char* ob = lds + wid * 8192 + lane16;
#pragma unroll
    for (int e = 0; e < 2; ++e) {
      const f32x16& t = ki ? st.o[e] : st.o[2 + e];
#pragma unroll
      for (int c = 0; c < 4; ++c) *(LAS f32x4*)(ob + (e * 4 + c) * 1024) = (f32x4){t[4 * c], t[4 * c + 1], t[4 * c + 2], t[4 * c + 3]};
    }
    WAIT_LGKM0(); BAR(); SBAR();
    const f32x2 o2 = ml[(wid ^ 4) * 32 + r32];
    const float M = fmaxf(st.mhat, o2[0]), a = __builtin_amdgcn_exp2f(st.mhat - M), b = __builtin_amdgcn_exp2f(o2[0] - M);
    const float inv = 1.f / (a * lt + b * o2[1]), sa = a * inv, sb = b * inv;
    LAS const char* pb = lds + (wid ^ 4) * 8192 + lane16;
#pragma unroll
    for (int e = 0; e < 2; ++e) {
      f32x16 t = ki ? st.o[2 + e] : st.o[e];
#pragma unroll
      for (int c = 0; c < 4; ++c) {
        const f32x4 v = *(LAS const f32x4*)(pb + (e * 4 + c) * 1024);
#pragma unroll
        for (int j = 0; j < 4; ++j) t[4 * c + j] = t[4 * c + j] * sa + v[j] * sb;
      }
      ctx_store(ctx, t, 1.f, blkH * 128 + qi * 32 + r32, head, 2 * ki + e, hi);
    }
  }
  asm volatile("s_waitcnt vmcnt(0)" ::: "memory");
  WAIT_LGKM0(); BAR(); SBAR();
  if (tid == 0) {
    flag_set(tg + TG_CTX + blkH * 32 + head, tag);
    if (T1 == 16) flag_set(tg + TG_CTX + blkL * 32 + head, tag);
  }
  STAMP(ws, 0, 9);
  back_phases(lds, x, ws, out, tag STAMP_ARGS);
  STAMP_DUMP(ws, 0, 0);
  STAMP_DUMP(ws, 3, 4 + (xcd & 1));
  STAMP_DUMP(ws, 4, 4 + ((xcd & 1) ^ 1));
  CODE_END(".Lall");
}

extern "C" void kernel_launch(void* const* d_in, const int* in_sizes, int n_in, void* d_out, int out_size, void* d_ws, size_t ws_size,
                              hipStream_t stream) {
  const u16* x = (const u16*)d_in[0]; const u16* ckv = (const u16*)d_in[1]; const u16* wq = (const u16*)d_in[2];
  const u16* wuk = (const u16*)d_in[3]; const u16* wuv = (const u16*)d_in[4]; const u16* wo = (const u16*)d_in[5];
  const u16* w1 = (const u16*)d_in[7]; const u16* w2 = (const u16*)d_in[8];
  char* ws = (char*)d_ws + ((ws_size - WS_END) & ~(size_t)(2 * MiB - 1));
  static bool attr = false;
  if (!attr) { attr = true; (void)hipFuncSetAttribute((const void*)k_all, hipFuncAttributeMaxDynamicSharedMemorySize, K2_LDS); }
  k_all<<<256, 512, K2_LDS, stream>>>(x, ckv, wq, wuk, wuv, wo, w1, w2, ws, (u16*)d_out);
}
```
